```python
import torch

def _softmax(x, axis=-1):
    return torch.softmax(x, dim=axis)
def _gelu(x):
    return torch.nn.functional.gelu(x, approximate="tanh")
def _silu(x):
    return torch.nn.functional.silu(x)
def _sigmoid(x):
    return torch.sigmoid(x)
def _layernorm(x, axis=-1, eps=1e-5):
    m = x.mean(dim=axis, keepdim=True)
    v = x.var(dim=axis, keepdim=True, unbiased=False)
    return (x - m) / torch.sqrt(v + eps)
def _scatter_add(tgt, idx, src, axis=0):
    return tgt.clone().index_add_(axis, idx, src)
def _segment_sum(src, seg, n):
    out = torch.zeros((n,) + src.shape[1:], dtype=src.dtype)
    return out.index_add_(0, seg, src)
def _pad(x, pw):
    flat = tuple(v for pair in reversed(pw) for v in pair)
    return torch.nn.functional.pad(x, flat)

kernel_name = 'concat_heads__gqa_block_cached__mha_block_cached__near_ridge'

def setup_inputs():

    _w = (lambda a: (lambda t: t.to(torch.bfloat16) if t.is_floating_point() else t)(torch.as_tensor(a)))
    def _A_setup():
        B, Sq, D = 16, 32, 640
        Hq, Hkv, Sk, Dh = 32, 32, 32, 192
        assert Hq % Hkv == 0, (Hq, Hkv)
        return {
            "x": _w((lambda *s: torch.randn(*s, dtype=torch.bfloat16) * 0.8)(B, Sq, D)),
            "k_cache": _w((lambda *s: torch.randn(*s, dtype=torch.bfloat16) * 0.8)(B, Hkv, Sk, Dh)),
            "v_cache": _w((lambda *s: torch.randn(*s, dtype=torch.bfloat16) * 0.05)(B, Hkv, Sk, Dh)),
            "wq": _w((lambda *s: torch.randn(*s, dtype=torch.bfloat16) * 0.8)(D, Hq * Dh) * (D ** -0.5)),
            "wo": _w((lambda *s: torch.randn(*s, dtype=torch.bfloat16) * 0.05)(Hq * Dh, D) * ((Hq * Dh) ** -0.5)),
        }
    _A_in = _A_setup()

    def _B_setup():
        B, Sq, D = 16, 32, 640
        H, Sk, Dh = 32, 32, 192
        return {
            "x": _w((lambda *s: torch.randn(*s, dtype=torch.bfloat16) * 0.8)(B, Sq, D)),
            "k_cache": _w((lambda *s: torch.randn(*s, dtype=torch.bfloat16) * 0.8)(B, H, Sk, Dh)),
            "v_cache": _w((lambda *s: torch.randn(*s, dtype=torch.bfloat16) * 0.05)(B, H, Sk, Dh)),
            "wq": _w((lambda *s: torch.randn(*s, dtype=torch.bfloat16) * 0.8)(D, H * Dh) * (D ** -0.5)),
            "wo": _w((lambda *s: torch.randn(*s, dtype=torch.bfloat16) * 0.05)(H * Dh, D) * ((H * Dh) ** -0.5)),
        }
    _B_in = _B_setup()

    return {
        "A_x": _A_in["x"],
        "A_k_cache": _A_in["k_cache"],
        "A_v_cache": _A_in["v_cache"],
        "A_wq": _A_in["wq"],
        "A_wo": _A_in["wo"],
        "B_x": _B_in["x"],
        "B_k_cache": _B_in["k_cache"],
        "B_v_cache": _B_in["v_cache"],
        "B_wq": _B_in["wq"],
        "B_wo": _B_in["wo"],
    }

def reference(A_x, A_k_cache, A_v_cache, A_wq, A_wo, B_x, B_k_cache, B_v_cache, B_wq, B_wo):
    def _A_ref(x, k_cache, v_cache, wq, wo):
        B, Sq, D = x.shape
        Hq, Dh = 32, 192
        Hkv = k_cache.shape[1]
        rep = Hq // Hkv
        q = (lambda x, *d: x.permute(*d))((x @ wq).reshape(B, Sq, Hq, Dh), 0, 2, 1, 3)
        k = (lambda x, n, axis: torch.repeat_interleave(x, n, dim=axis))(k_cache, rep, axis=1)
        v = (lambda x, n, axis: torch.repeat_interleave(x, n, dim=axis))(v_cache, rep, axis=1)
        scores = torch.einsum("bhqd,bhkd->bhqk", q, k) * (Dh ** -0.5)
        p = _softmax(scores, axis=-1)
        o = torch.einsum("bhqk,bhkd->bhqd", p, v)
        return (lambda x, *d: x.permute(*d))(o, 0, 2, 1, 3).reshape(B, Sq, Hq * Dh) @ wo

    def _B_ref(x, k_cache, v_cache, wq, wo):
        B, Sq, D = x.shape
        H, Dh = 32, 192
        q = (lambda x, *d: x.permute(*d))((x @ wq).reshape(B, Sq, H, Dh), 0, 2, 1, 3)
        scores = torch.einsum("bhqd,bhkd->bhqk", q, k_cache) * (Dh ** -0.5)
        p = _softmax(scores, axis=-1)
        o = torch.einsum("bhqk,bhkd->bhqd", p, v_cache)
        return (lambda x, *d: x.permute(*d))(o, 0, 2, 1, 3).reshape(B, Sq, H * Dh) @ wo

    a = _A_ref(A_x, A_k_cache, A_v_cache, A_wq, A_wo)
    b = _B_ref(B_x, B_k_cache, B_v_cache, B_wq, B_wo)
    return torch.concatenate([a, b], axis=-1)
```

```cpp
#include <hip/hip_runtime.h>
#include <hip/hip_bf16.h>
typedef unsigned short u16;
__device__ __forceinline__ float b2f(u16 v){ return __uint_as_float(((unsigned)v)<<16); }
__device__ __forceinline__ u16 f2b(float f){ __hip_bfloat16 h = __float2bfloat16(f); return *reinterpret_cast<u16*>(&h); }
__global__ void k_attn(const u16* xA,const u16* kA,const u16* vA,const u16* wqA,
                       const u16* xB,const u16* kB,const u16* vB,const u16* wqB, u16* O){
  int id = blockIdx.x; int br = id/512; int b=(id%512)/32; int h=id%32;
  const u16* x = (br?xB:xA) + b*32*640; const u16* kc=(br?kB:kA)+(size_t)(b*32+h)*32*192;
  const u16* vc=(br?vB:vA)+(size_t)(b*32+h)*32*192; const u16* wq=(br?wqB:wqA)+h*192;
  __shared__ float q[32*192]; __shared__ float s[32*32];
  for(int e=threadIdx.x;e<32*192;e+=256){ int r=e/192,c=e%192; float a=0; for(int k=0;k<640;++k) a+=b2f(x[r*640+k])*b2f(wq[(size_t)k*6144+c]); q[e]=b2f(f2b(a)); }
  __syncthreads();
  for(int e=threadIdx.x;e<1024;e+=256){ int r=e/32,c=e%32; float a=0; for(int k=0;k<192;++k) a+=q[r*192+k]*b2f(kc[c*192+k]); a=b2f(f2b(a)); a=b2f(f2b(a*0.07216878364870322f)); s[e]=a; }
  __syncthreads();
  if(threadIdx.x<32){ int r=threadIdx.x; float m=-1e30f; for(int c=0;c<32;++c) m=fmaxf(m,s[r*32+c]); float sum=0; for(int c=0;c<32;++c){ float e=__expf(s[r*32+c]-m); s[r*32+c]=e; sum+=e;} for(int c=0;c<32;++c) s[r*32+c]=b2f(f2b(s[r*32+c]/sum)); }
  __syncthreads();
  for(int e=threadIdx.x;e<32*192;e+=256){ int r=e/192,c=e%192; float a=0; for(int k=0;k<32;++k) a+=s[r*32+k]*b2f(vc[k*192+c]); O[((size_t)br*512+b*32+r)*6144+h*192+c]=f2b(a); }
}
__global__ void k_out(const u16* O,const u16* woA,const u16* woB,u16* out){
  int e=blockIdx.x*256+threadIdx.x; int br=e/(512*640); int r=(e%(512*640))/640; int c=e%640;
  const u16* wo=br?woB:woA; const u16* o=O+((size_t)br*512+r)*6144; float a=0;
  for(int k=0;k<6144;++k) a+=b2f(o[k])*b2f(wo[(size_t)k*640+c]);
  out[(size_t)r*1280+br*640+c]=f2b(a);
}
extern "C" void kernel_launch(void* const* d_in, const int* in_sizes, int n_in, void* d_out, int out_size, void* d_ws, size_t ws_size, hipStream_t stream) {
  u16* O=(u16*)d_ws;
  k_attn<<<1024,256,0,stream>>>((const u16*)d_in[0],(const u16*)d_in[1],(const u16*)d_in[2],(const u16*)d_in[3],(const u16*)d_in[5],(const u16*)d_in[6],(const u16*)d_in[7],(const u16*)d_in[8],O);
  k_out<<<2*512*640/256,256,0,stream>>>(O,(const u16*)d_in[4],(const u16*)d_in[9],(u16*)d_out);
}
```

```cpp
#include <hip/hip_runtime.h>
#include <hip/hip_bf16.h>

typedef unsigned short u16;
typedef unsigned int u32;
typedef unsigned long long u64;
typedef short s16x4 __attribute__((ext_vector_type(4)));
typedef short s16x8 __attribute__((ext_vector_type(8)));
typedef __bf16 bf16x8 __attribute__((ext_vector_type(8)));
typedef __bf16 bf16x2 __attribute__((ext_vector_type(2)));
typedef float f32x2 __attribute__((ext_vector_type(2)));
typedef float f32x16 __attribute__((ext_vector_type(16)));
typedef u32 u32x4 __attribute__((ext_vector_type(4)));
#define LAS __attribute__((address_space(3)))
#define GAS __attribute__((address_space(1)))
#define DI __device__ __forceinline__

constexpr u32 WS_G    = 0;
constexpr u32 WS_FP   = 32768;
constexpr u32 WS_FR   = 65536;
constexpr u32 WS_P    = 131072;
constexpr u32 WS_IN   = WS_P + 1024 * 2048;
constexpr u32 WS_END  = WS_IN + 256 * 8 * 5120;

constexpr u32 SLOT = 49152;
constexpr u32 LDS_BYTES = 3 * SLOT;
constexpr u32 SPIN_CAP = 1u << 18;

struct Args { const u16* in[10]; u16* out; char* ws; };

DI u64 mix64(u64 g) { return g * 0x9E3779B97F4A7C15ull + 0x632BE59BD9B4E019ull; }
DI u32 pk2(float lo, float hi) { f32x2 t = {lo, hi}; return __builtin_bit_cast(u32, __builtin_convertvector(t, bf16x2)); }
DI bf16x8 pack8(const f32x16& x, int s) {
  u32x4 p = {pk2(x[8*s], x[8*s+1]), pk2(x[8*s+2], x[8*s+3]), pk2(x[8*s+4], x[8*s+5]), pk2(x[8*s+6], x[8*s+7])};
  return __builtin_bit_cast(bf16x8, p);
}
DI bf16x8 lds_row(u32 addr) { return __builtin_bit_cast(bf16x8, *(LAS s16x8*)(addr)); }
DI bf16x8 lds_tr(u32 a0, u32 a1) {
  s16x4 lo = __builtin_amdgcn_ds_read_tr16_b64_v4i16((LAS s16x4*)(a0));
  s16x4 hi = __builtin_amdgcn_ds_read_tr16_b64_v4i16((LAS s16x4*)(a1));
  return __builtin_bit_cast(bf16x8, __builtin_shufflevector(lo, hi, 0, 1, 2, 3, 4, 5, 6, 7));
}
#define MFMA(a, b, c) __builtin_amdgcn_mfma_f32_32x32x16_bf16((a), (b), (c), 0, 0, 0)
#define BAR_C() asm volatile("s_waitcnt lgkmcnt(0)\n\ts_barrier" ::: "memory")
#define BAR_L(n) asm volatile("s_waitcnt vmcnt(%0)\n\ts_barrier" ::"n"(n) : "memory")
#define DMA(voff, sbase, ldsbase, imm) \
  asm volatile("s_add_u32 m0, %2, %3\n\ts_nop 0\n\tglobal_load_lds_dwordx4 %0, %1" ::"v"(voff), "s"(sbase), "s"(ldsbase), "n"(imm) : "memory", "scc")

#define STAMP(i) do {} while (0)

__global__ __launch_bounds__(512, 2) void fused_attn_block(Args a) {
  const int bid = blockIdx.x;
  const int xcd = bid & 7, idx = bid >> 3;
  const int br = xcd >> 2, hh8 = idx >> 2, rg = idx & 3;
  const int h = (xcd & 3) * 8 + hh8, hg = h >> 2, ct = h & 3;
  const int wid = __builtin_amdgcn_readfirstlane(threadIdx.x >> 6);
  const int lane = threadIdx.x & 63;
  const int self = br * 128 + h * 4 + rg;
  STAMP(0);
  const __amdgpu_buffer_rsrc_t wsr = __builtin_amdgcn_make_buffer_rsrc(a.ws, 0, WS_END, 0x00020000);

  u64 gv;
  {
    const int w = wid & 3;
    int tgt = self;
    if (lane < 4) tgt = br * 128 + (hg * 4 + lane) * 4 + rg;
    else if (lane < 6) tgt = br * 128 + ((2 * w + lane - 4) * 4 + ct) * 4 + rg;
    gv = __hip_atomic_load((GAS u64*)(a.ws + WS_G + tgt * 128), __ATOMIC_RELAXED, __HIP_MEMORY_SCOPE_AGENT);
  }

  if (wid >= 4) {
    const int lw = wid - 4;
    const char* xg = (const char*)a.in[br * 5 + 0] + rg * (128 * 1280);
    const char* kg = (const char*)a.in[br * 5 + 1] + (size_t)((rg * 4 + lw) * 32 + h) * 12288;
    const char* vg = (const char*)a.in[br * 5 + 2] + (size_t)((rg * 4 + lw) * 32 + hg * 4) * 12288;
    const char* wq = (const char*)a.in[br * 5 + 3] + h * 384;
    const char* wo = (const char*)a.in[br * 5 + 4] + (size_t)(hg * 768 + lw * 16) * 1280 + ct * 320;
    const u32 r8 = lane >> 3, pos = lane & 7;
    u32 vw[6], vx[4];
#pragma unroll
    for (int u = 0; u < 6; ++u) vw[u] = ((lw * 2 + u / 3) * 8 + r8) * 12288 + (u % 3) * 128 + ((pos ^ (((r8 >> 1) & 1) << 2)) << 4);
#pragma unroll
    for (int u = 0; u < 4; ++u) vx[u] = ((lw * 4 + u) * 8 + r8) * 1280 + ((pos ^ (4 * (u & 1) + (r8 >> 1))) << 4);
    u32 slot = 0;
    auto issue1 = [&]() {
      const u32 lb = slot + lw * 6144, lx = slot + 24576 + lw * 4096;
      DMA(vw[0], wq, lb, 0); DMA(vw[1], wq, lb, 1024); DMA(vw[2], wq, lb, 2048);
      DMA(vx[0], xg, lx, 0); DMA(vx[1], xg, lx, 1024);
      DMA(vw[3], wq, lb, 3072); DMA(vw[4], wq, lb, 4096); DMA(vw[5], wq, lb, 5120);
      DMA(vx[2], xg, lx, 2048); DMA(vx[3], xg, lx, 3072);
      wq += 64 * 12288; xg += 128;
      slot = slot == 2 * SLOT ? 0 : slot + SLOT;
    };
    issue1(); issue1();
    u32 vk[4], vo[5];
#pragma unroll
    for (int u = 0; u < 4; ++u) vk[u] = (u * 8 + r8) * 384 + ((pos ^ (4 * (u & 1) + (r8 >> 1))) << 4);
#pragma unroll
    for (int u = 0; u < 5; ++u) { const u32 id = u * 1024 + lane * 16; vo[u] = (id / 320) * 1280 + id % 320; }
    auto issue2 = [&]() {
      const u32 lv = slot + lw * 4096, lo = slot + 16384 + lw * 5120;
      DMA(vk[0], vg, lv, 0); DMA(vk[1], vg, lv, 1024);
      DMA(vo[0], wo, lo, 0); DMA(vo[1], wo, lo, 1024); DMA(vo[2], wo, lo, 2048);
      DMA(vk[2], vg, lv, 2048); DMA(vk[3], vg, lv, 3072);
      DMA(vo[3], wo, lo, 3072); DMA(vo[4], wo, lo, 4096);
      wo += 64 * 1280;
      slot = slot == 2 * SLOT ? 0 : slot + SLOT;
    };
#pragma unroll 1
    for (int s = 0; s < 8; ++s) { BAR_L(10); issue1(); }
    BAR_L(10);
    {
      const u32 lk = slot + lw * 12288;
#pragma unroll
      for (int cb = 0; cb < 3; ++cb) {
        const char* kk = kg + cb * 128;
        DMA(vk[0], kk, lk, cb * 4096); DMA(vk[1], kk, lk, cb * 4096 + 1024);
        DMA(vk[2], kk, lk, cb * 4096 + 2048); DMA(vk[3], kk, lk, cb * 4096 + 3072);
      }
      slot = slot == 2 * SLOT ? 0 : slot + SLOT;
    }
    BAR_L(12); issue2(); vg += 128;
    int third = 1;
#pragma unroll 1
    for (int s = 10; s < 21; ++s) {
      BAR_L(9); issue2();
      vg += third == 2 ? 12288 - 256 : 128; third = third == 2 ? 0 : third + 1;
    }
    BAR_L(9);
    BAR_L(0);
    STAMP(1);
  } else {
    const int w = wid;
    const u32 hf = lane >> 5, blk = (lane >> 4) & 1, q4 = (lane & 15) >> 2, p4 = lane & 3, r32 = lane & 31;
    const u32 colp = 32 * blk + 16 * (p4 & 1) + 8 * (p4 >> 1);
    const u32 wl0 = hf * 3072 + q4 * 128 + ((q4 >> 1) * 64) + colp;
    const u32 wl1 = wl0 ^ 64;
    const u32 rowl = r32 * 128 + ((hf ^ ((r32 >> 1) & 7)) << 4);
    u32 slot = 0;
    f32x16 q[6];
#pragma unroll
    for (int c = 0; c < 6; ++c) q[c] = f32x16{};
#pragma unroll 1
    for (int s = 0; s < 10; ++s) {
      BAR_C();
      const u32 a0 = slot + wl0, a1 = slot + wl1, xa = slot + 24576 + w * 4096 + rowl;
#pragma unroll
      for (int ks = 0; ks < 4; ++ks) {
        const bf16x8 xb = lds_row(xa ^ (ks << 5));
#pragma unroll
        for (int c = 0; c < 6; ++c) {
          const u32 ad = ((c & 1) ? a1 : a0) + ks * 6144 + (c >> 1) * 1024;
          q[c] = MFMA(lds_tr(ad, ad + 512), xb, q[c]);
        }
      }
      slot = slot == 2 * SLOT ? 0 : slot + SLOT;
    }
    STAMP(1);
    BAR_C();
    bf16x8 pf[2];
    {
      f32x16 sc = f32x16{};
      const u32 ka = slot + w * 12288 + rowl;
#pragma unroll
      for (int c = 0; c < 6; ++c)
#pragma unroll
        for (int s = 0; s < 2; ++s)
          sc = MFMA(lds_row((ka + (c >> 1) * 4096) ^ ((2 * (c & 1) + s) << 5)), pack8(q[c], s), sc);
      slot = slot == 2 * SLOT ? 0 : slot + SLOT;
      float m = sc[0];
#pragma unroll
      for (int i = 1; i < 16; ++i) m = fmaxf(m, sc[i]);
      { auto r = __builtin_amdgcn_permlane32_swap(__float_as_uint(m), __float_as_uint(m), false, false);
        m = fmaxf(__uint_as_float(r[0]), __uint_as_float(r[1])); }
      const float cs = 0.07216878364870322f * 1.4426950408889634f;
      float sum = 0.f;
#pragma unroll
      for (int i = 0; i < 16; ++i) { sc[i] = __builtin_amdgcn_exp2f((sc[i] - m) * cs); sum += sc[i]; }
      { auto r = __builtin_amdgcn_permlane32_swap(__float_as_uint(sum), __float_as_uint(sum), false, false);
        sum = __uint_as_float(r[0]) + __uint_as_float(r[1]); }
      const float inv = 1.0f / sum;
#pragma unroll
      for (int i = 0; i < 16; ++i) sc[i] *= inv;
      const u32 po = WS_P + ((br * 16 + rg * 4 + w) * 32 + h) * 2048 + lane * 16;
      __builtin_amdgcn_raw_buffer_store_b128(__builtin_bit_cast(u32x4, pack8(sc, 0)), wsr, po, 0, 16);
      __builtin_amdgcn_raw_buffer_store_b128(__builtin_bit_cast(u32x4, pack8(sc, 1)), wsr, po + 1024, 0, 16);
    }
    const u64 sig = mix64(gv);
    const u64 want = ((u64)__builtin_amdgcn_readlane((u32)(sig >> 32), 8) << 32) | (u32)__builtin_amdgcn_readlane((u32)sig, 8);
    asm volatile("s_waitcnt vmcnt(0)" ::: "memory");
    if (lane < 4)
      __hip_atomic_store((GAS u64*)(a.ws + WS_FP + (br * 128 + (hg * 4 + lane) * 4 + rg) * 128 + (ct * 4 + w) * 8), sig,
                         __ATOMIC_RELAXED, __HIP_MEMORY_SCOPE_AGENT);
    STAMP(2);

    f32x16 o[5];
#pragma unroll
    for (int c = 0; c < 5; ++c) o[c] = f32x16{};
    const u32 ol = (8 * hf + q4) * 320 + colp + 16384;
#pragma unroll 1
    for (int hx = 0; hx < 4; ++hx) {
      f32x16 t[5];
#pragma unroll
      for (int c = 0; c < 5; ++c) t[c] = f32x16{};
#pragma unroll
      for (int st = 0; st < 3; ++st) {
        BAR_C();
        if (st == 2) {
          if (hx == 0) {
            const GAS u64* fp = (const GAS u64*)(a.ws + WS_FP + self * 128 + ((lane & 3) * 4 + w) * 8);
            for (u32 spin = 0; spin < SPIN_CAP; ++spin) {
              const u64 f = __hip_atomic_load(fp, __ATOMIC_RELAXED, __HIP_MEMORY_SCOPE_AGENT);
              if (__all(f == want)) break;
              __builtin_amdgcn_s_sleep(2);
            }
            __builtin_amdgcn_fence(__ATOMIC_ACQUIRE, "wavefront");
          }
          const u32 po = WS_P + ((br * 16 + rg * 4 + w) * 32 + hg * 4 + hx) * 2048 + lane * 16;
          pf[0] = __builtin_bit_cast(bf16x8, __builtin_amdgcn_raw_buffer_load_b128(wsr, po, 0, 16));
          pf[1] = __builtin_bit_cast(bf16x8, __builtin_amdgcn_raw_buffer_load_b128(wsr, po + 1024, 0, 16));
        }
        const u32 va = slot + w * 4096 + rowl, oa = slot + ol;
#pragma unroll
        for (int ks = 0; ks < 4; ++ks) {
          const bf16x8 vb = lds_row(va ^ (ks << 5));
#pragma unroll
          for (int c = 0; c < 5; ++c) {
            const u32 ad = oa + ks * 5120 + c * 64;
            t[c] = MFMA(vb, lds_tr(ad, ad + 1280), t[c]);
          }
        }
        slot = slot == 2 * SLOT ? 0 : slot + SLOT;
      }
#pragma unroll
      for (int c = 0; c < 5; ++c)
#pragma unroll
        for (int s = 0; s < 2; ++s) o[c] = MFMA(pack8(t[c], s), pf[s], o[c]);
    }
    STAMP(3);
    {
      const u32 owner = br * 128 + ((2 * w + blk) * 4 + ct) * 4 + rg;
      const u32 io = WS_IN + (owner * 8 + hg) * 5120 + (hf * 16 + (lane & 15)) * 16;
#pragma unroll
      for (int c = 0; c < 5; ++c)
#pragma unroll
        for (int s = 0; s < 2; ++s)
          __builtin_amdgcn_raw_buffer_store_b128(__builtin_bit_cast(u32x4, pack8(o[c], s)), wsr, io + (2 * c + s) * 512, 0, 16);
      asm volatile("s_waitcnt vmcnt(0)" ::: "memory");
      if (lane == 4 || lane == 5)
        __hip_atomic_store((GAS u64*)(a.ws + WS_FR + (br * 128 + ((2 * w + lane - 4) * 4 + ct) * 4 + rg) * 128 + hg * 8), sig,
                           __ATOMIC_RELAXED, __HIP_MEMORY_SCOPE_AGENT);
    }
    STAMP(4);
  }

  if (wid >= 3) {
    const int c = wid - 3;
    const u64 sig = mix64(gv);
    const u64 want = ((u64)__builtin_amdgcn_readlane((u32)(sig >> 32), 8) << 32) | (u32)__builtin_amdgcn_readlane((u32)sig, 8);
    const GAS u64* fr = (const GAS u64*)(a.ws + WS_FR + self * 128 + (lane & 7) * 8);
    for (u32 spin = 0; spin < SPIN_CAP; ++spin) {
      const u64 f = __hip_atomic_load(fr, __ATOMIC_RELAXED, __HIP_MEMORY_SCOPE_AGENT);
      if (__all(f == want)) break;
      __builtin_amdgcn_s_sleep(2);
    }
    __builtin_amdgcn_fence(__ATOMIC_ACQUIRE, "wavefront");
    STAMP(5);
    const u32 io = WS_IN + self * 8 * 5120 + c * 1024 + lane * 16;
    u32x4 v[8];
#pragma unroll
    for (int g = 0; g < 8; ++g) v[g] = __builtin_amdgcn_raw_buffer_load_b128(wsr, io + g * 5120, 0, 16);
    float acc[8];
#pragma unroll
    for (int j = 0; j < 8; ++j) acc[j] = 0.f;
#pragma unroll
    for (int g = 0; g < 8; ++g)
#pragma unroll
      for (int j = 0; j < 4; ++j) {
        acc[2 * j] += __uint_as_float(v[g][j] << 16);
        acc[2 * j + 1] += __uint_as_float(v[g][j] & 0xffff0000u);
      }
    const u32x4 r = {pk2(acc[0], acc[1]), pk2(acc[2], acc[3]), pk2(acc[4], acc[5]), pk2(acc[6], acc[7])};
    const int row = rg * 128 + hg * 16 + (lane & 15);
    const int col = br * 640 + ct * 160 + c * 32 + (lane >> 5) * 16 + ((lane >> 4) & 1) * 8;
    *(u32x4*)(a.out + row * 1280 + col) = r;
    if (wid == 4 && lane == 8)
      __hip_atomic_store((GAS u64*)(a.ws + WS_G + self * 128), sig, __ATOMIC_RELAXED, __HIP_MEMORY_SCOPE_AGENT);
    STAMP(6);
  }
}

extern "C" void kernel_launch(void* const* d_in, const int* in_sizes, int n_in, void* d_out, int out_size,
                              void* d_ws, size_t ws_size, hipStream_t stream) {
  Args a;
  for (int i = 0; i < 10; ++i) a.in[i] = (const u16*)d_in[i];
  a.out = (u16*)d_out;
  a.ws = (char*)d_ws;
  (void)hipFuncSetAttribute((const void*)fused_attn_block, hipFuncAttributeMaxDynamicSharedMemorySize, LDS_BYTES);
  fused_attn_block<<<256, 512, LDS_BYTES, stream>>>(a);
}
```

```cpp
#define POL_W " nt"
#define POL_K " nt"
#define POL_V " nt"
#define POL_O " nt"
#include <hip/hip_runtime.h>
#include <hip/hip_bf16.h>

typedef unsigned short u16;
typedef unsigned int u32;
typedef unsigned long long u64;
typedef short s16x4 __attribute__((ext_vector_type(4)));
typedef short s16x8 __attribute__((ext_vector_type(8)));
typedef __bf16 bf16x8 __attribute__((ext_vector_type(8)));
typedef __bf16 bf16x2 __attribute__((ext_vector_type(2)));
typedef float f32x2 __attribute__((ext_vector_type(2)));
typedef float f32x16 __attribute__((ext_vector_type(16)));
typedef u32 u32x4 __attribute__((ext_vector_type(4)));
#define LAS __attribute__((address_space(3)))
#define GAS __attribute__((address_space(1)))
#define DI __device__ __forceinline__

constexpr u32 WS_G    = 0;
constexpr u32 WS_FP   = 32768;
constexpr u32 WS_FR   = 65536;
constexpr u32 WS_P    = 131072;
constexpr u32 WS_IN   = WS_P + 1024 * 2048;
constexpr u32 WS_END  = WS_IN + 256 * 8 * 5120;

constexpr u32 SLOT = 49152;
constexpr u32 LDS_BYTES = 3 * SLOT;
constexpr u32 SPIN_CAP = 1u << 18;

struct Args { const u16* in[10]; u16* out; char* ws; };

DI u64 mix64(u64 g) { return g * 0x9E3779B97F4A7C15ull + 0x632BE59BD9B4E019ull; }
DI u32 pk2(float lo, float hi) { f32x2 t = {lo, hi}; return __builtin_bit_cast(u32, __builtin_convertvector(t, bf16x2)); }
DI bf16x8 pack8(const f32x16& x, int s) {
  u32x4 p = {pk2(x[8*s], x[8*s+1]), pk2(x[8*s+2], x[8*s+3]), pk2(x[8*s+4], x[8*s+5]), pk2(x[8*s+6], x[8*s+7])};
  return __builtin_bit_cast(bf16x8, p);
}
DI bf16x8 lds_row(u32 addr) { return __builtin_bit_cast(bf16x8, *(LAS s16x8*)(addr)); }
DI bf16x8 lds_tr(u32 a0, u32 a1) {
  s16x4 lo = __builtin_amdgcn_ds_read_tr16_b64_v4i16((LAS s16x4*)(a0));
  s16x4 hi = __builtin_amdgcn_ds_read_tr16_b64_v4i16((LAS s16x4*)(a1));
  return __builtin_bit_cast(bf16x8, __builtin_shufflevector(lo, hi, 0, 1, 2, 3, 4, 5, 6, 7));
}
#define MFMA(a, b, c) __builtin_amdgcn_mfma_f32_32x32x16_bf16((a), (b), (c), 0, 0, 0)
#define OPAQUE(x) asm volatile("" : "+v"(x))
#define SGB(m, n) __builtin_amdgcn_sched_group_barrier(m, n, 0)
#define BAR_C() asm volatile("s_waitcnt lgkmcnt(0)\n\ts_barrier" ::: "memory")
#define BAR_L(n) asm volatile("s_waitcnt vmcnt(%0)\n\ts_barrier" ::"n"(n) : "memory")
#define DMAP(pol, voff, sbase, ldsbase, imm) \
  asm volatile("s_add_u32 m0, %2, %3\n\ts_nop 0\n\tglobal_load_lds_dwordx4 %0, %1" pol ::"v"(voff), "s"(sbase), "s"(ldsbase), "n"(imm) : "memory", "scc")
#define POL_X ""

#define STAMP(i) do {} while (0)
#define STAMP_DUMP() do {} while (0)

__global__ __launch_bounds__(512, 2) void fused_attn_block(Args a) {
  const int bid = blockIdx.x;
  const int xcd = bid & 7, idx = bid >> 3;
  const int br = xcd >> 2, hh8 = idx >> 2, rg = idx & 3;
  const int h = (xcd & 3) * 8 + hh8, hg = h >> 2, ct = h & 3;
  const int wid = __builtin_amdgcn_readfirstlane(threadIdx.x >> 6);
  const int lane = threadIdx.x & 63;
  const int self = br * 128 + h * 4 + rg;
  STAMP(0);
  const __amdgpu_buffer_rsrc_t wsr = __builtin_amdgcn_make_buffer_rsrc(a.ws, 0, WS_END, 0x00020000);

  u64 gv;
  {
    const int w = wid & 3;
    int tgt = self;
    if (lane < 4) tgt = br * 128 + (hg * 4 + lane) * 4 + rg;
    else if (lane < 6) tgt = br * 128 + ((2 * w + lane - 4) * 4 + ct) * 4 + rg;
    gv = __hip_atomic_load((GAS u64*)(a.ws + WS_G + tgt * 128), __ATOMIC_RELAXED, __HIP_MEMORY_SCOPE_AGENT);
  }

  if (wid >= 4) {
    const int lw = wid - 4;
    const char* xg = (const char*)a.in[br * 5 + 0] + rg * (128 * 1280);
    const char* kg = (const char*)a.in[br * 5 + 1] + (size_t)((rg * 4 + lw) * 32 + h) * 12288;
    const char* vg = (const char*)a.in[br * 5 + 2] + (size_t)((rg * 4 + lw) * 32 + hg * 4) * 12288;
    const char* wq = (const char*)a.in[br * 5 + 3] + h * 384;
    const char* wo = (const char*)a.in[br * 5 + 4] + (size_t)(hg * 768 + lw * 16) * 1280 + ct * 320;
    const u32 r8 = lane >> 3, pos = lane & 7;
    u32 vw[6], vx[4];
#pragma unroll
    for (int u = 0; u < 6; ++u) vw[u] = ((lw * 2 + u / 3) * 8 + r8) * 12288 + (u % 3) * 128 + ((pos ^ (((r8 >> 1) & 1) << 2)) << 4);
#pragma unroll
    for (int u = 0; u < 4; ++u) vx[u] = ((lw * 4 + u) * 8 + r8) * 1280 + ((pos ^ (4 * (u & 1) + (r8 >> 1))) << 4);
    u32 slot = 0;
    auto issue1 = [&]() {
      const u32 lb = slot + lw * 6144, lx = slot + 24576 + lw * 4096;
      DMAP(POL_W, vw[0], wq, lb, 0); DMAP(POL_W, vw[1], wq, lb, 1024); DMAP(POL_W, vw[2], wq, lb, 2048);
      DMAP(POL_X, vx[0], xg, lx, 0); DMAP(POL_X, vx[1], xg, lx, 1024);
      DMAP(POL_W, vw[3], wq, lb, 3072); DMAP(POL_W, vw[4], wq, lb, 4096); DMAP(POL_W, vw[5], wq, lb, 5120);
      DMAP(POL_X, vx[2], xg, lx, 2048); DMAP(POL_X, vx[3], xg, lx, 3072);
      wq += 64 * 12288; xg += 128;
      slot = slot == 2 * SLOT ? 0 : slot + SLOT;
    };
    issue1(); issue1();
    u32 vk[4], vo[5];
#pragma unroll
    for (int u = 0; u < 4; ++u) vk[u] = (u * 8 + r8) * 384 + ((pos ^ (4 * (u & 1) + (r8 >> 1))) << 4);
#pragma unroll
    for (int u = 0; u < 5; ++u) { const u32 id = u * 1024 + lane * 16; vo[u] = (id / 320) * 1280 + id % 320; }
    auto issue2 = [&]() {
      const u32 lv = slot + lw * 4096, lo = slot + 16384 + lw * 5120;
      DMAP(POL_V, vk[0], vg, lv, 0); DMAP(POL_V, vk[1], vg, lv, 1024);
      DMAP(POL_O, vo[0], wo, lo, 0); DMAP(POL_O, vo[1], wo, lo, 1024); DMAP(POL_O, vo[2], wo, lo, 2048);
      DMAP(POL_V, vk[2], vg, lv, 2048); DMAP(POL_V, vk[3], vg, lv, 3072);
      DMAP(POL_O, vo[3], wo, lo, 3072); DMAP(POL_O, vo[4], wo, lo, 4096);
      wo += 64 * 1280;
      slot = slot == 2 * SLOT ? 0 : slot + SLOT;
    };
#pragma unroll 1
    for (int s = 0; s < 8; ++s) { STAMP(32 + s); BAR_L(10); STAMP(8 + s); issue1(); }
    STAMP(40); BAR_L(10); STAMP(16);
    {
      const u32 lk = slot + lw * 12288;
#pragma unroll
      for (int cb = 0; cb < 3; ++cb) {
        const char* kk = kg + cb * 128;
        DMAP(POL_K, vk[0], kk, lk, cb * 4096); DMAP(POL_K, vk[1], kk, lk, cb * 4096 + 1024);
        DMAP(POL_K, vk[2], kk, lk, cb * 4096 + 2048); DMAP(POL_K, vk[3], kk, lk, cb * 4096 + 3072);
      }
      slot = slot == 2 * SLOT ? 0 : slot + SLOT;
    }
    STAMP(41); BAR_L(12); STAMP(17); issue2(); vg += 128;
    int third = 1;
#pragma unroll 1
    for (int s = 10; s < 21; ++s) {
      STAMP(32 + s); BAR_L(9); STAMP(8 + s); issue2();
      vg += third == 2 ? 12288 - 256 : 128; third = third == 2 ? 0 : third + 1;
    }
    STAMP(53); BAR_L(9); STAMP(29);
    BAR_L(0); STAMP(30);
    STAMP(1);
  } else {
    const int w = wid;
    const u32 hf = lane >> 5, blk = (lane >> 4) & 1, q4 = (lane & 15) >> 2, p4 = lane & 3, r32 = lane & 31;
    const u32 colp = 32 * blk + 16 * (p4 & 1) + 8 * (p4 >> 1);
    const u32 wl0 = hf * 3072 + q4 * 128 + ((q4 >> 1) * 64) + colp;
    const u32 wl1 = wl0 ^ 64;
    const u32 rowl = r32 * 128 + ((hf ^ ((r32 >> 1) & 7)) << 4);
    u32 slot = 0;
    f32x16 q[6];
#pragma unroll
    for (int c = 0; c < 6; ++c) q[c] = f32x16{};
    bf16x8 fa[2][6], fb[2];
#pragma unroll
    for (int c = 0; c < 6; ++c) fa[1][c] = bf16x8{};
    fb[1] = bf16x8{};
#pragma unroll 1
    for (int s = 0; s < 10; ++s) {
      u32 a0 = slot + wl0, a1 = slot + wl1, xa = slot + 24576 + w * 4096 + rowl;
      OPAQUE(a0); OPAQUE(a1); OPAQUE(xa);
      STAMP(32 + s); BAR_C(); STAMP(8 + s);
#pragma unroll
      for (int ks = 0; ks < 4; ++ks) {
        fb[ks & 1] = lds_row(xa ^ (ks << 5));
#pragma unroll
        for (int c = 0; c < 6; ++c) {
          const u32 ad = ((c & 1) ? a1 : a0) + ks * 6144 + (c >> 1) * 1024;
          fa[ks & 1][c] = lds_tr(ad, ad + 512);
        }
#pragma unroll
        for (int c = 0; c < 6; ++c) q[c] = MFMA(fa[(ks & 1) ^ 1][c], fb[(ks & 1) ^ 1], q[c]);
        SGB(0x100, 3); SGB(0x8, 1);
#pragma unroll
        for (int c = 1; c < 6; ++c) { SGB(0x100, 2); SGB(0x8, 1); }
        __builtin_amdgcn_sched_barrier(0);
      }
      slot = slot == 2 * SLOT ? 0 : slot + SLOT;
    }
    STAMP(1);
    STAMP(42); BAR_C(); STAMP(18);
    bf16x8 pf[2];
    {
      f32x16 sc = f32x16{};
      const u32 ka = slot + w * 12288 + rowl;
      bf16x8 kf[12];
#pragma unroll
      for (int i = 0; i < 12; ++i) kf[i] = lds_row((ka + (i >> 2) * 4096) ^ ((i & 3) << 5));
#pragma unroll
      for (int c = 0; c < 6; ++c) q[c] = MFMA(fa[1][c], fb[1], q[c]);
      __builtin_amdgcn_sched_barrier(0);
#pragma unroll
      for (int c = 0; c < 6; ++c)
#pragma unroll
        for (int s = 0; s < 2; ++s) sc = MFMA(kf[2 * c + s], pack8(q[c], s), sc);
      slot = slot == 2 * SLOT ? 0 : slot + SLOT;
      float m = sc[0];
#pragma unroll
      for (int i = 1; i < 16; ++i) m = fmaxf(m, sc[i]);
      { auto r = __builtin_amdgcn_permlane32_swap(__float_as_uint(m), __float_as_uint(m), false, false);
        m = fmaxf(__uint_as_float(r[0]), __uint_as_float(r[1])); }
      const float cs = 0.07216878364870322f * 1.4426950408889634f;
      float sum = 0.f;
#pragma unroll
      for (int i = 0; i < 16; ++i) { sc[i] = __builtin_amdgcn_exp2f((sc[i] - m) * cs); sum += sc[i]; }
      { auto r = __builtin_amdgcn_permlane32_swap(__float_as_uint(sum), __float_as_uint(sum), false, false);
        sum = __uint_as_float(r[0]) + __uint_as_float(r[1]); }
      const float inv = 1.0f / sum;
#pragma unroll
      for (int i = 0; i < 16; ++i) sc[i] *= inv;
      const u32 po = WS_P + ((br * 16 + rg * 4 + w) * 32 + h) * 2048 + lane * 16;
      __builtin_amdgcn_raw_buffer_store_b128(__builtin_bit_cast(u32x4, pack8(sc, 0)), wsr, po, 0, 16);
      __builtin_amdgcn_raw_buffer_store_b128(__builtin_bit_cast(u32x4, pack8(sc, 1)), wsr, po + 1024, 0, 16);
    }
    const u64 sig = mix64(gv);
    const u64 want = ((u64)__builtin_amdgcn_readlane((u32)(sig >> 32), 8) << 32) | (u32)__builtin_amdgcn_readlane((u32)sig, 8);
    asm volatile("s_waitcnt vmcnt(0)" ::: "memory");
    if (lane < 4)
      __hip_atomic_store((GAS u64*)(a.ws + WS_FP + (br * 128 + (hg * 4 + lane) * 4 + rg) * 128 + (ct * 4 + w) * 8), sig,
                         __ATOMIC_RELAXED, __HIP_MEMORY_SCOPE_AGENT);
    STAMP(2);

    f32x16 o[5], t[5];
#pragma unroll
    for (int c = 0; c < 5; ++c) o[c] = f32x16{};
    const u32 ol = (8 * hf + q4) * 320 + colp + 16384;
    bf16x8 gb[2][5], ga[2];
#pragma unroll 1
    for (int hx = 0; hx < 4; ++hx) {
#pragma unroll
      for (int st = 0; st < 3; ++st) {
        u32 va = slot + w * 4096 + rowl, oa = slot + ol;
        OPAQUE(va); OPAQUE(oa);
        STAMP(43 + hx * 3 + st); BAR_C(); STAMP(19 + hx * 3 + st);
        if (st == 2) {
          if (hx == 0) {
            const GAS u64* fp = (const GAS u64*)(a.ws + WS_FP + self * 128 + ((lane & 3) * 4 + w) * 8);
#pragma unroll 1
            for (u32 spin = 0; spin < SPIN_CAP; ++spin) {
              const u64 f = __hip_atomic_load(fp, __ATOMIC_RELAXED, __HIP_MEMORY_SCOPE_AGENT);
              if (__all(f == want)) break;
              __builtin_amdgcn_s_sleep(2);
            }
            __builtin_amdgcn_fence(__ATOMIC_ACQUIRE, "wavefront");
          }
          const u32 po = WS_P + ((br * 16 + rg * 4 + w) * 32 + hg * 4 + hx) * 2048 + lane * 16;
          pf[0] = __builtin_bit_cast(bf16x8, __builtin_amdgcn_raw_buffer_load_b128(wsr, po, 0, 16));
          pf[1] = __builtin_bit_cast(bf16x8, __builtin_amdgcn_raw_buffer_load_b128(wsr, po + 1024, 0, 16));
          __builtin_amdgcn_sched_barrier(0);
        }
#pragma unroll
        for (int ks = 0; ks < 4; ++ks) {
          ga[ks & 1] = lds_row(va ^ (ks << 5));
#pragma unroll
          for (int c = 0; c < 5; ++c) {
            const u32 ad = oa + ks * 5120 + c * 64;
            gb[ks & 1][c] = lds_tr(ad, ad + 1280);
          }
          if (st == 0 && ks == 0) {
            __builtin_amdgcn_sched_barrier(0);
            if (hx) {
#pragma unroll
              for (int c = 0; c < 5; ++c) t[c] = MFMA(ga[1], gb[1][c], t[c]);
#pragma unroll
              for (int c = 0; c < 5; ++c)
#pragma unroll
                for (int s = 0; s < 2; ++s) o[c] = MFMA(pack8(t[c], s), pf[s], o[c]);
            }
          } else {
#pragma unroll
            for (int c = 0; c < 5; ++c)
              t[c] = MFMA(ga[(ks & 1) ^ 1], gb[(ks & 1) ^ 1][c], (st == 0 && ks == 1) ? f32x16{} : t[c]);
            SGB(0x100, 3); SGB(0x8, 1);
#pragma unroll
            for (int c = 1; c < 5; ++c) { SGB(0x100, 2); SGB(0x8, 1); }
          }
          __builtin_amdgcn_sched_barrier(0);
        }
        slot = slot == 2 * SLOT ? 0 : slot + SLOT;
      }
    }
#pragma unroll
    for (int c = 0; c < 5; ++c) t[c] = MFMA(ga[1], gb[1][c], t[c]);
#pragma unroll
    for (int c = 0; c < 5; ++c)
#pragma unroll
      for (int s = 0; s < 2; ++s) o[c] = MFMA(pack8(t[c], s), pf[s], o[c]);
    STAMP(3);
    {
      const u32 owner = br * 128 + ((2 * w + blk) * 4 + ct) * 4 + rg;
      const u32 io = WS_IN + (owner * 8 + hg) * 5120 + (hf * 16 + (lane & 15)) * 16;
#pragma unroll
      for (int c = 0; c < 5; ++c)
#pragma unroll
        for (int s = 0; s < 2; ++s)
          __builtin_amdgcn_raw_buffer_store_b128(__builtin_bit_cast(u32x4, pack8(o[c], s)), wsr, io + (2 * c + s) * 512, 0, 16);
      asm volatile("s_waitcnt vmcnt(0)" ::: "memory");
      if (lane == 4 || lane == 5)
        __hip_atomic_store((GAS u64*)(a.ws + WS_FR + (br * 128 + ((2 * w + lane - 4) * 4 + ct) * 4 + rg) * 128 + hg * 8), sig,
                           __ATOMIC_RELAXED, __HIP_MEMORY_SCOPE_AGENT);
    }
    STAMP(4);
  }

  if (wid >= 3) {
    const int c = wid - 3;
    const u64 sig = mix64(gv);
    const u64 want = ((u64)__builtin_amdgcn_readlane((u32)(sig >> 32), 8) << 32) | (u32)__builtin_amdgcn_readlane((u32)sig, 8);
    const GAS u64* fr = (const GAS u64*)(a.ws + WS_FR + self * 128 + (lane & 7) * 8);
#pragma unroll 1
    for (u32 spin = 0; spin < SPIN_CAP; ++spin) {
      const u64 f = __hip_atomic_load(fr, __ATOMIC_RELAXED, __HIP_MEMORY_SCOPE_AGENT);
      if (__all(f == want)) break;
      __builtin_amdgcn_s_sleep(2);
    }
    __builtin_amdgcn_fence(__ATOMIC_ACQUIRE, "wavefront");
    STAMP(5);
    const u32 io = WS_IN + self * 8 * 5120 + c * 1024 + lane * 16;
    u32x4 v[8];
#pragma unroll
    for (int g = 0; g < 8; ++g) v[g] = __builtin_amdgcn_raw_buffer_load_b128(wsr, io + g * 5120, 0, 16);
    float acc[8];
#pragma unroll
    for (int j = 0; j < 8; ++j) acc[j] = 0.f;
#pragma unroll
    for (int g = 0; g < 8; ++g)
#pragma unroll
      for (int j = 0; j < 4; ++j) {
        acc[2 * j] += __uint_as_float(v[g][j] << 16);
        acc[2 * j + 1] += __uint_as_float(v[g][j] & 0xffff0000u);
      }
    const u32x4 r = {pk2(acc[0], acc[1]), pk2(acc[2], acc[3]), pk2(acc[4], acc[5]), pk2(acc[6], acc[7])};
    const int row = rg * 128 + hg * 16 + (lane & 15);
    const int col = br * 640 + ct * 160 + c * 32 + (lane >> 5) * 16 + ((lane >> 4) & 1) * 8;
    *(u32x4*)(a.out + row * 1280 + col) = r;
    if (wid == 4 && lane == 8)
      __hip_atomic_store((GAS u64*)(a.ws + WS_G + self * 128), sig, __ATOMIC_RELAXED, __HIP_MEMORY_SCOPE_AGENT);
    STAMP(6);
  }
  STAMP_DUMP();
}

extern "C" void kernel_launch(void* const* d_in, const int* in_sizes, int n_in, void* d_out, int out_size,
                              void* d_ws, size_t ws_size, hipStream_t stream) {
  Args a;
  for (int i = 0; i < 10; ++i) a.in[i] = (const u16*)d_in[i];
  a.out = (u16*)d_out;
  a.ws = (char*)d_ws;
  (void)hipFuncSetAttribute((const void*)fused_attn_block, hipFuncAttributeMaxDynamicSharedMemorySize, LDS_BYTES);
  fused_attn_block<<<256, 512, LDS_BYTES, stream>>>(a);
}
```

```cpp
#define PROBE 8
#define PROBE_W 4
#include <hip/hip_runtime.h>
#include <hip/hip_bf16.h>

typedef unsigned short u16;
typedef unsigned int u32;
typedef unsigned long long u64;
typedef short s16x4 __attribute__((ext_vector_type(4)));
typedef short s16x8 __attribute__((ext_vector_type(8)));
typedef __bf16 bf16x8 __attribute__((ext_vector_type(8)));
typedef __bf16 bf16x2 __attribute__((ext_vector_type(2)));
typedef float f32x2 __attribute__((ext_vector_type(2)));
typedef float f32x16 __attribute__((ext_vector_type(16)));
typedef u32 u32x4 __attribute__((ext_vector_type(4)));
#define LAS __attribute__((address_space(3)))
#define GAS __attribute__((address_space(1)))
#define DI __device__ __forceinline__

constexpr u32 WS_G    = 0;
constexpr u32 WS_FP   = 32768;
constexpr u32 WS_FR   = 65536;
constexpr u32 WS_P    = 131072;
constexpr u32 WS_IN   = WS_P + 1024 * 2048;
constexpr u32 WS_END  = WS_IN + 256 * 8 * 5120;

constexpr u32 SLOT = 49152;
constexpr u32 LDS_BYTES = 3 * SLOT;
constexpr u32 SPIN_CAP = 1u << 18;

struct Args { const u16* in[10]; u16* out; char* ws; };

DI u64 mix64(u64 g) { return g * 0x9E3779B97F4A7C15ull + 0x632BE59BD9B4E019ull; }
DI u32 pk2(float lo, float hi) { f32x2 t = {lo, hi}; return __builtin_bit_cast(u32, __builtin_convertvector(t, bf16x2)); }
DI bf16x8 pack8(const f32x16& x, int s) {
  u32x4 p = {pk2(x[8*s], x[8*s+1]), pk2(x[8*s+2], x[8*s+3]), pk2(x[8*s+4], x[8*s+5]), pk2(x[8*s+6], x[8*s+7])};
  return __builtin_bit_cast(bf16x8, p);
}
DI bf16x8 lds_row(u32 addr) { return __builtin_bit_cast(bf16x8, *(LAS s16x8*)(addr)); }
DI bf16x8 lds_tr(u32 a0, u32 a1) {
  s16x4 lo = __builtin_amdgcn_ds_read_tr16_b64_v4i16((LAS s16x4*)(a0));
  s16x4 hi = __builtin_amdgcn_ds_read_tr16_b64_v4i16((LAS s16x4*)(a1));
  return __builtin_bit_cast(bf16x8, __builtin_shufflevector(lo, hi, 0, 1, 2, 3, 4, 5, 6, 7));
}
#define MFMA(a, b, c) __builtin_amdgcn_mfma_f32_32x32x16_bf16((a), (b), (c), 0, 0, 0)
#define OPAQUE(x) asm volatile("" : "+v"(x))
#define SGB(m, n) __builtin_amdgcn_sched_group_barrier(m, n, 0)
#define BAR_C() asm volatile("s_waitcnt lgkmcnt(0)\n\ts_barrier" ::: "memory")
#define BAR_L(n) asm volatile("s_waitcnt vmcnt(%0)\n\ts_barrier" ::"n"(n) : "memory")
#define DMAP(pol, voff, sbase, ldsbase, imm) \
  asm volatile("s_add_u32 m0, %2, %3\n\ts_nop 0\n\tglobal_load_lds_dwordx4 %0, %1" pol ::"v"(voff), "s"(sbase), "s"(ldsbase), "n"(imm) : "memory", "scc")
#define POL_W ""
#define POL_X ""
#define POL_K ""
#define POL_V ""
#define POL_O ""

#define STAMP(i) do { if ((i) == 0) probe0 = __builtin_amdgcn_s_memrealtime(); if ((i) == PROBE) probe1 = __builtin_amdgcn_s_memrealtime(); } while (0)
#define STAMP_DUMP() do { if (bid == 100 && wid == PROBE_W) { const u64 te = __builtin_amdgcn_s_memrealtime(); const u64 d = (probe1 - probe0) * 8; while (__builtin_amdgcn_s_memrealtime() - te < d) __builtin_amdgcn_s_sleep(4); } } while (0)

__global__ __launch_bounds__(512, 2) void fused_attn_block(Args a) {
  const int bid = blockIdx.x;
  const int xcd = bid & 7, idx = bid >> 3;
  const int br = xcd >> 2, hh8 = idx >> 2, rg = idx & 3;
  const int h = (xcd & 3) * 8 + hh8, hg = h >> 2, ct = h & 3;
  const int wid = __builtin_amdgcn_readfirstlane(threadIdx.x >> 6);
  const int lane = threadIdx.x & 63;
  const int self = br * 128 + h * 4 + rg;
  u64 probe0 = 0, probe1 = 0;
  STAMP(0);
  const __amdgpu_buffer_rsrc_t wsr = __builtin_amdgcn_make_buffer_rsrc(a.ws, 0, WS_END, 0x00020000);

  u64 gv;
  {
    const int w = wid & 3;
    int tgt = self;
    if (lane < 4) tgt = br * 128 + (hg * 4 + lane) * 4 + rg;
    else if (lane < 6) tgt = br * 128 + ((2 * w + lane - 4) * 4 + ct) * 4 + rg;
    gv = __hip_atomic_load((GAS u64*)(a.ws + WS_G + tgt * 128), __ATOMIC_RELAXED, __HIP_MEMORY_SCOPE_AGENT);
  }

  if (wid >= 4) {
    const int lw = wid - 4;
    const char* xg = (const char*)a.in[br * 5 + 0] + rg * (128 * 1280);
    const char* kg = (const char*)a.in[br * 5 + 1] + (size_t)((rg * 4 + lw) * 32 + h) * 12288;
    const char* vg = (const char*)a.in[br * 5 + 2] + (size_t)((rg * 4 + lw) * 32 + hg * 4) * 12288;
    const char* wq = (const char*)a.in[br * 5 + 3] + h * 384;
    const char* wo = (const char*)a.in[br * 5 + 4] + (size_t)(hg * 768 + lw * 16) * 1280 + ct * 320;
    const u32 r8 = lane >> 3, pos = lane & 7;
    u32 vw[6], vx[4];
#pragma unroll
    for (int u = 0; u < 6; ++u) vw[u] = ((lw * 2 + u / 3) * 8 + r8) * 12288 + (u % 3) * 128 + ((pos ^ (((r8 >> 1) & 1) << 2)) << 4);
#pragma unroll
    for (int u = 0; u < 4; ++u) vx[u] = ((lw * 4 + u) * 8 + r8) * 1280 + ((pos ^ (4 * (u & 1) + (r8 >> 1))) << 4);
    u32 slot = 0;
    auto issue1 = [&]() {
      const u32 lb = slot + lw * 6144, lx = slot + 24576 + lw * 4096;
      DMAP(POL_W, vw[0], wq, lb, 0); DMAP(POL_W, vw[1], wq, lb, 1024); DMAP(POL_W, vw[2], wq, lb, 2048);
      DMAP(POL_X, vx[0], xg, lx, 0); DMAP(POL_X, vx[1], xg, lx, 1024);
      DMAP(POL_W, vw[3], wq, lb, 3072); DMAP(POL_W, vw[4], wq, lb, 4096); DMAP(POL_W, vw[5], wq, lb, 5120);
      DMAP(POL_X, vx[2], xg, lx, 2048); DMAP(POL_X, vx[3], xg, lx, 3072);
      wq += 64 * 12288; xg += 128;
      slot = slot == 2 * SLOT ? 0 : slot + SLOT;
    };
    issue1(); issue1();
    u32 vk[4], vo[5];
#pragma unroll
    for (int u = 0; u < 4; ++u) vk[u] = (u * 8 + r8) * 384 + ((pos ^ (4 * (u & 1) + (r8 >> 1))) << 4);
#pragma unroll
    for (int u = 0; u < 5; ++u) { const u32 id = u * 1024 + lane * 16; vo[u] = (id / 320) * 1280 + id % 320; }
    auto issue2 = [&]() {
      const u32 lv = slot + lw * 4096, lo = slot + 16384 + lw * 5120;
      DMAP(POL_V, vk[0], vg, lv, 0); DMAP(POL_V, vk[1], vg, lv, 1024);
      DMAP(POL_O, vo[0], wo, lo, 0); DMAP(POL_O, vo[1], wo, lo, 1024); DMAP(POL_O, vo[2], wo, lo, 2048);
      DMAP(POL_V, vk[2], vg, lv, 2048); DMAP(POL_V, vk[3], vg, lv, 3072);
      DMAP(POL_O, vo[3], wo, lo, 3072); DMAP(POL_O, vo[4], wo, lo, 4096);
      wo += 64 * 1280;
      slot = slot == 2 * SLOT ? 0 : slot + SLOT;
    };
#pragma unroll 1
    for (int s = 0; s < 8; ++s) { STAMP(32 + s); BAR_L(10); STAMP(8 + s); issue1(); }
    STAMP(40); BAR_L(10); STAMP(16);
    {
      const u32 lk = slot + lw * 12288;
#pragma unroll
      for (int cb = 0; cb < 3; ++cb) {
        const char* kk = kg + cb * 128;
        DMAP(POL_K, vk[0], kk, lk, cb * 4096); DMAP(POL_K, vk[1], kk, lk, cb * 4096 + 1024);
        DMAP(POL_K, vk[2], kk, lk, cb * 4096 + 2048); DMAP(POL_K, vk[3], kk, lk, cb * 4096 + 3072);
      }
      slot = slot == 2 * SLOT ? 0 : slot + SLOT;
    }
    STAMP(41); BAR_L(12); STAMP(17); issue2(); vg += 128;
    int third = 1;
#pragma unroll 1
    for (int s = 10; s < 21; ++s) {
      STAMP(32 + s); BAR_L(9); STAMP(8 + s); issue2();
      vg += third == 2 ? 12288 - 256 : 128; third = third == 2 ? 0 : third + 1;
    }
    STAMP(53); BAR_L(9); STAMP(29);
    BAR_L(0); STAMP(30);
    STAMP(1);
  } else {
    const int w = wid;
    const u32 hf = lane >> 5, blk = (lane >> 4) & 1, q4 = (lane & 15) >> 2, p4 = lane & 3, r32 = lane & 31;
    const u32 colp = 32 * blk + 16 * (p4 & 1) + 8 * (p4 >> 1);
    const u32 wl0 = hf * 3072 + q4 * 128 + ((q4 >> 1) * 64) + colp;
    const u32 wl1 = wl0 ^ 64;
    const u32 rowl = r32 * 128 + ((hf ^ ((r32 >> 1) & 7)) << 4);
    u32 slot = 0;
    f32x16 q[6];
#pragma unroll
    for (int c = 0; c < 6; ++c) q[c] = f32x16{};
    bf16x8 fa[2][6], fb[2];
#pragma unroll
    for (int c = 0; c < 6; ++c) fa[1][c] = bf16x8{};
    fb[1] = bf16x8{};
#pragma unroll 1
    for (int s = 0; s < 10; ++s) {
      u32 a0 = slot + wl0, a1 = slot + wl1, xa = slot + 24576 + w * 4096 + rowl;
      OPAQUE(a0); OPAQUE(a1); OPAQUE(xa);
      BAR_C();
#pragma unroll
      for (int ks = 0; ks < 4; ++ks) {
        fb[ks & 1] = lds_row(xa ^ (ks << 5));
#pragma unroll
        for (int c = 0; c < 6; ++c) {
          const u32 ad = ((c & 1) ? a1 : a0) + ks * 6144 + (c >> 1) * 1024;
          fa[ks & 1][c] = lds_tr(ad, ad + 512);
        }
#pragma unroll
        for (int c = 0; c < 6; ++c) q[c] = MFMA(fa[(ks & 1) ^ 1][c], fb[(ks & 1) ^ 1], q[c]);
        SGB(0x100, 3); SGB(0x8, 1);
#pragma unroll
        for (int c = 1; c < 6; ++c) { SGB(0x100, 2); SGB(0x8, 1); }
        __builtin_amdgcn_sched_barrier(0);
      }
      slot = slot == 2 * SLOT ? 0 : slot + SLOT;
    }
    STAMP(1);
    BAR_C();
    bf16x8 pf[2];
    {
      f32x16 sc = f32x16{};
      const u32 ka = slot + w * 12288 + rowl;
      bf16x8 kf[12];
#pragma unroll
      for (int i = 0; i < 12; ++i) kf[i] = lds_row((ka + (i >> 2) * 4096) ^ ((i & 3) << 5));
#pragma unroll
      for (int c = 0; c < 6; ++c) q[c] = MFMA(fa[1][c], fb[1], q[c]);
      __builtin_amdgcn_sched_barrier(0);
#pragma unroll
      for (int c = 0; c < 6; ++c)
#pragma unroll
        for (int s = 0; s < 2; ++s) sc = MFMA(kf[2 * c + s], pack8(q[c], s), sc);
      slot = slot == 2 * SLOT ? 0 : slot + SLOT;
      float m = sc[0];
#pragma unroll
      for (int i = 1; i < 16; ++i) m = fmaxf(m, sc[i]);
      { auto r = __builtin_amdgcn_permlane32_swap(__float_as_uint(m), __float_as_uint(m), false, false);
        m = fmaxf(__uint_as_float(r[0]), __uint_as_float(r[1])); }
      const float cs = 0.07216878364870322f * 1.4426950408889634f;
      float sum = 0.f;
#pragma unroll
      for (int i = 0; i < 16; ++i) { sc[i] = __builtin_amdgcn_exp2f((sc[i] - m) * cs); sum += sc[i]; }
      { auto r = __builtin_amdgcn_permlane32_swap(__float_as_uint(sum), __float_as_uint(sum), false, false);
        sum = __uint_as_float(r[0]) + __uint_as_float(r[1]); }
      const float inv = 1.0f / sum;
#pragma unroll
      for (int i = 0; i < 16; ++i) sc[i] *= inv;
      const u32 po = WS_P + ((br * 16 + rg * 4 + w) * 32 + h) * 2048 + lane * 16;
      __builtin_amdgcn_raw_buffer_store_b128(__builtin_bit_cast(u32x4, pack8(sc, 0)), wsr, po, 0, 16);
      __builtin_amdgcn_raw_buffer_store_b128(__builtin_bit_cast(u32x4, pack8(sc, 1)), wsr, po + 1024, 0, 16);
    }
    const u64 sig = mix64(gv);
    const u64 want = ((u64)__builtin_amdgcn_readlane((u32)(sig >> 32), 8) << 32) | (u32)__builtin_amdgcn_readlane((u32)sig, 8);
    asm volatile("s_waitcnt vmcnt(0)" ::: "memory");
    if (lane < 4)
      __hip_atomic_store((GAS u64*)(a.ws + WS_FP + (br * 128 + (hg * 4 + lane) * 4 + rg) * 128 + (ct * 4 + w) * 8), sig,
                         __ATOMIC_RELAXED, __HIP_MEMORY_SCOPE_AGENT);
    STAMP(2);

    f32x16 o[5], t[5];
#pragma unroll
    for (int c = 0; c < 5; ++c) o[c] = f32x16{};
    const u32 ol = (8 * hf + q4) * 320 + colp + 16384;
    bf16x8 gb[2][5], ga[2];
#pragma unroll 1
    for (int hx = 0; hx < 4; ++hx) {
#pragma unroll
      for (int st = 0; st < 3; ++st) {
        u32 va = slot + w * 4096 + rowl, oa = slot + ol;
        OPAQUE(va); OPAQUE(oa);
        BAR_C();
        if (st == 2) {
          if (hx == 0) {
            const GAS u64* fp = (const GAS u64*)(a.ws + WS_FP + self * 128 + ((lane & 3) * 4 + w) * 8);
#pragma unroll 1
            for (u32 spin = 0; spin < SPIN_CAP; ++spin) {
              const u64 f = __hip_atomic_load(fp, __ATOMIC_RELAXED, __HIP_MEMORY_SCOPE_AGENT);
              if (__all(f == want)) break;
              __builtin_amdgcn_s_sleep(2);
            }
            __builtin_amdgcn_fence(__ATOMIC_ACQUIRE, "wavefront");
          }
          const u32 po = WS_P + ((br * 16 + rg * 4 + w) * 32 + hg * 4 + hx) * 2048 + lane * 16;
          pf[0] = __builtin_bit_cast(bf16x8, __builtin_amdgcn_raw_buffer_load_b128(wsr, po, 0, 16));
          pf[1] = __builtin_bit_cast(bf16x8, __builtin_amdgcn_raw_buffer_load_b128(wsr, po + 1024, 0, 16));
          __builtin_amdgcn_sched_barrier(0);
        }
#pragma unroll
        for (int ks = 0; ks < 4; ++ks) {
          ga[ks & 1] = lds_row(va ^ (ks << 5));
#pragma unroll
          for (int c = 0; c < 5; ++c) {
            const u32 ad = oa + ks * 5120 + c * 64;
            gb[ks & 1][c] = lds_tr(ad, ad + 1280);
          }
          if (st == 0 && ks == 0) {
            __builtin_amdgcn_sched_barrier(0);
            if (hx) {
#pragma unroll
              for (int c = 0; c < 5; ++c) t[c] = MFMA(ga[1], gb[1][c], t[c]);
#pragma unroll
              for (int c = 0; c < 5; ++c)
#pragma unroll
                for (int s = 0; s < 2; ++s) o[c] = MFMA(pack8(t[c], s), pf[s], o[c]);
            }
          } else {
#pragma unroll
            for (int c = 0; c < 5; ++c)
              t[c] = MFMA(ga[(ks & 1) ^ 1], gb[(ks & 1) ^ 1][c], (st == 0 && ks == 1) ? f32x16{} : t[c]);
            SGB(0x100, 3); SGB(0x8, 1);
#pragma unroll
            for (int c = 1; c < 5; ++c) { SGB(0x100, 2); SGB(0x8, 1); }
          }
          __builtin_amdgcn_sched_barrier(0);
        }
        slot = slot == 2 * SLOT ? 0 : slot + SLOT;
      }
    }
#pragma unroll
    for (int c = 0; c < 5; ++c) t[c] = MFMA(ga[1], gb[1][c], t[c]);
#pragma unroll
    for (int c = 0; c < 5; ++c)
#pragma unroll
      for (int s = 0; s < 2; ++s) o[c] = MFMA(pack8(t[c], s), pf[s], o[c]);
    STAMP(3);
    {
      const u32 owner = br * 128 + ((2 * w + blk) * 4 + ct) * 4 + rg;
      const u32 io = WS_IN + (owner * 8 + hg) * 5120 + (hf * 16 + (lane & 15)) * 16;
#pragma unroll
      for (int c = 0; c < 5; ++c)
#pragma unroll
        for (int s = 0; s < 2; ++s)
          __builtin_amdgcn_raw_buffer_store_b128(__builtin_bit_cast(u32x4, pack8(o[c], s)), wsr, io + (2 * c + s) * 512, 0, 16);
      asm volatile("s_waitcnt vmcnt(0)" ::: "memory");
      if (lane == 4 || lane == 5)
        __hip_atomic_store((GAS u64*)(a.ws + WS_FR + (br * 128 + ((2 * w + lane - 4) * 4 + ct) * 4 + rg) * 128 + hg * 8), sig,
                           __ATOMIC_RELAXED, __HIP_MEMORY_SCOPE_AGENT);
    }
    STAMP(4);
  }

  if (wid >= 3) {
    const int c = wid - 3;
    const u64 sig = mix64(gv);
    const u64 want = ((u64)__builtin_amdgcn_readlane((u32)(sig >> 32), 8) << 32) | (u32)__builtin_amdgcn_readlane((u32)sig, 8);
    const GAS u64* fr = (const GAS u64*)(a.ws + WS_FR + self * 128 + (lane & 7) * 8);
#pragma unroll 1
    for (u32 spin = 0; spin < SPIN_CAP; ++spin) {
      const u64 f = __hip_atomic_load(fr, __ATOMIC_RELAXED, __HIP_MEMORY_SCOPE_AGENT);
      if (__all(f == want)) break;
      __builtin_amdgcn_s_sleep(2);
    }
    __builtin_amdgcn_fence(__ATOMIC_ACQUIRE, "wavefront");
    STAMP(5);
    const u32 io = WS_IN + self * 8 * 5120 + c * 1024 + lane * 16;
    u32x4 v[8];
#pragma unroll
    for (int g = 0; g < 8; ++g) v[g] = __builtin_amdgcn_raw_buffer_load_b128(wsr, io + g * 5120, 0, 16);
    float acc[8];
#pragma unroll
    for (int j = 0; j < 8; ++j) acc[j] = 0.f;
#pragma unroll
    for (int g = 0; g < 8; ++g)
#pragma unroll
      for (int j = 0; j < 4; ++j) {
        acc[2 * j] += __uint_as_float(v[g][j] << 16);
        acc[2 * j + 1] += __uint_as_float(v[g][j] & 0xffff0000u);
      }
    const u32x4 r = {pk2(acc[0], acc[1]), pk2(acc[2], acc[3]), pk2(acc[4], acc[5]), pk2(acc[6], acc[7])};
    const int row = rg * 128 + hg * 16 + (lane & 15);
    const int col = br * 640 + ct * 160 + c * 32 + (lane >> 5) * 16 + ((lane >> 4) & 1) * 8;
    *(u32x4*)(a.out + row * 1280 + col) = r;
    if (wid == 4 && lane == 8)
      __hip_atomic_store((GAS u64*)(a.ws + WS_G + self * 128), sig, __ATOMIC_RELAXED, __HIP_MEMORY_SCOPE_AGENT);
    STAMP(6);
  }
  STAMP_DUMP();
}

extern "C" void kernel_launch(void* const* d_in, const int* in_sizes, int n_in, void* d_out, int out_size,
                              void* d_ws, size_t ws_size, hipStream_t stream) {
  Args a;
  for (int i = 0; i < 10; ++i) a.in[i] = (const u16*)d_in[i];
  a.out = (u16*)d_out;
  a.ws = (char*)d_ws;
  (void)hipFuncSetAttribute((const void*)fused_attn_block, hipFuncAttributeMaxDynamicSharedMemorySize, LDS_BYTES);
  fused_attn_block<<<256, 512, LDS_BYTES, stream>>>(a);
}
```

```cpp
#define PROBE2 0
#define PROBE_B 100
#include <hip/hip_runtime.h>
#include <hip/hip_bf16.h>

typedef unsigned short u16;
typedef unsigned int u32;
typedef unsigned long long u64;
typedef short s16x4 __attribute__((ext_vector_type(4)));
typedef short s16x8 __attribute__((ext_vector_type(8)));
typedef __bf16 bf16x8 __attribute__((ext_vector_type(8)));
typedef __bf16 bf16x2 __attribute__((ext_vector_type(2)));
typedef float f32x2 __attribute__((ext_vector_type(2)));
typedef float f32x16 __attribute__((ext_vector_type(16)));
typedef u32 u32x4 __attribute__((ext_vector_type(4)));
#define LAS __attribute__((address_space(3)))
#define GAS __attribute__((address_space(1)))
#define DI __device__ __forceinline__

constexpr u32 WS_G    = 0;
constexpr u32 WS_FP   = 32768;
constexpr u32 WS_FR   = 65536;
constexpr u32 WS_P    = 131072;
constexpr u32 WS_IN   = WS_P + 1024 * 2048;
constexpr u32 WS_END  = WS_IN + 256 * 8 * 5120;

constexpr u32 SLOT = 49152;
constexpr u32 LDS_BYTES = 3 * SLOT;
constexpr u32 SPIN_CAP = 1u << 18;

struct Args { const u16* in[10]; u16* out; char* ws; };

DI u64 mix64(u64 g) { return g * 0x9E3779B97F4A7C15ull + 0x632BE59BD9B4E019ull; }
DI u32 pk2(float lo, float hi) { f32x2 t = {lo, hi}; return __builtin_bit_cast(u32, __builtin_convertvector(t, bf16x2)); }
DI bf16x8 pack8(const f32x16& x, int s) {
  u32x4 p = {pk2(x[8*s], x[8*s+1]), pk2(x[8*s+2], x[8*s+3]), pk2(x[8*s+4], x[8*s+5]), pk2(x[8*s+6], x[8*s+7])};
  return __builtin_bit_cast(bf16x8, p);
}
DI bf16x8 lds_row(u32 addr) { return __builtin_bit_cast(bf16x8, *(LAS s16x8*)(addr)); }
DI bf16x8 lds_tr(u32 a0, u32 a1) {
  s16x4 lo = __builtin_amdgcn_ds_read_tr16_b64_v4i16((LAS s16x4*)(a0));
  s16x4 hi = __builtin_amdgcn_ds_read_tr16_b64_v4i16((LAS s16x4*)(a1));
  return __builtin_bit_cast(bf16x8, __builtin_shufflevector(lo, hi, 0, 1, 2, 3, 4, 5, 6, 7));
}
#define MFMA(a, b, c) __builtin_amdgcn_mfma_f32_32x32x16_bf16((a), (b), (c), 0, 0, 0)
#define OPAQUE(x) asm volatile("" : "+v"(x))
#define SGB(m, n) __builtin_amdgcn_sched_group_barrier(m, n, 0)
#define BAR_C() asm volatile("s_waitcnt lgkmcnt(0)\n\ts_barrier" ::: "memory")
#define BAR_L(n) asm volatile("s_waitcnt vmcnt(%0)\n\ts_barrier" ::"n"(n) : "memory")
#define DMAP(pol, voff, sbase, ldsbase, imm) \
  asm volatile("s_add_u32 m0, %2, %3\n\ts_nop 0\n\tglobal_load_lds_dwordx4 %0, %1" pol ::"v"(voff), "s"(sbase), "s"(ldsbase), "n"(imm) : "memory", "scc")
#define POL_W ""
#define POL_X ""
#define POL_K ""
#define POL_V ""
#define POL_O ""

#define PROBE 0
#define STAMP(i) do { if ((i) == 0) probe0 = __builtin_amdgcn_s_memrealtime(); } while (0)
#define STAMP_DUMP() do { if (wid == 4) { GAS u64* pb = (GAS u64*)(a.ws + (64u << 20)); \
    if (lane == 0) { __hip_atomic_store(pb + bid * 2, probe0, __ATOMIC_RELAXED, __HIP_MEMORY_SCOPE_AGENT); __hip_atomic_store(pb + bid * 2 + 1, (u64)__builtin_amdgcn_s_memrealtime(), __ATOMIC_RELAXED, __HIP_MEMORY_SCOPE_AGENT); } \
    if (bid == PROBE_B) { while (__builtin_amdgcn_s_memrealtime() - probe0 < 6000) __builtin_amdgcn_s_sleep(4); \
      int mn = 1 << 30, mxs = -(1 << 30), mxe = -(1 << 30); \
      for (int i = 0; i < 4; ++i) { const int s0 = (int)(__hip_atomic_load(pb + (lane * 4 + i) * 2, __ATOMIC_RELAXED, __HIP_MEMORY_SCOPE_AGENT) - probe0); const int e0 = (int)(__hip_atomic_load(pb + (lane * 4 + i) * 2 + 1, __ATOMIC_RELAXED, __HIP_MEMORY_SCOPE_AGENT) - probe0); \
        mn = min(mn, s0); mxs = max(mxs, s0); mxe = max(mxe, e0); } \
      for (int o = 32; o; o >>= 1) { mn = min(mn, __shfl_xor(mn, o)); mxs = max(mxs, __shfl_xor(mxs, o)); mxe = max(mxe, __shfl_xor(mxe, o)); } \
      int d = PROBE2 == 0 ? 0 : PROBE2 == 1 ? mxe - mn : PROBE2 == 2 ? mxs - mn : PROBE2 == 3 ? -mn : mxe; d = min(max(d, 0), 10000); \
      while ((long long)(__builtin_amdgcn_s_memrealtime() - probe0) < 6200 + d * 8) __builtin_amdgcn_s_sleep(4); } } } while (0)

__global__ __launch_bounds__(512, 2) void fused_attn_block(Args a) {
  const int bid = blockIdx.x;
  const int xcd = bid & 7, idx = bid >> 3;
  const int br = xcd >> 2, hh8 = idx >> 2, rg = idx & 3;
  const int h = (xcd & 3) * 8 + hh8, hg = h >> 2, ct = h & 3;
  const int wid = __builtin_amdgcn_readfirstlane(threadIdx.x >> 6);
  const int lane = threadIdx.x & 63;
  const int self = br * 128 + h * 4 + rg;
  u64 probe0 = 0, probe1 = 0;
  STAMP(0);
  const __amdgpu_buffer_rsrc_t wsr = __builtin_amdgcn_make_buffer_rsrc(a.ws, 0, WS_END, 0x00020000);

  u64 gv;
  {
    const int w = wid & 3;
    int tgt = self;
    if (lane < 4) tgt = br * 128 + (hg * 4 + lane) * 4 + rg;
    else if (lane < 6) tgt = br * 128 + ((2 * w + lane - 4) * 4 + ct) * 4 + rg;
    gv = __hip_atomic_load((GAS u64*)(a.ws + WS_G + tgt * 128), __ATOMIC_RELAXED, __HIP_MEMORY_SCOPE_AGENT);
  }

  if (wid >= 4) {
    const int lw = wid - 4;
    const char* xg = (const char*)a.in[br * 5 + 0] + rg * (128 * 1280);
    const char* kg = (const char*)a.in[br * 5 + 1] + (size_t)((rg * 4 + lw) * 32 + h) * 12288;
    const char* vg = (const char*)a.in[br * 5 + 2] + (size_t)((rg * 4 + lw) * 32 + hg * 4) * 12288;
    const char* wq = (const char*)a.in[br * 5 + 3] + h * 384;
    const char* wo = (const char*)a.in[br * 5 + 4] + (size_t)(hg * 768 + lw * 16) * 1280 + ct * 320;
    const u32 r8 = lane >> 3, pos = lane & 7;
    u32 vw[6], vx[4];
#pragma unroll
    for (int u = 0; u < 6; ++u) vw[u] = ((lw * 2 + u / 3) * 8 + r8) * 12288 + (u % 3) * 128 + ((pos ^ (((r8 >> 1) & 1) << 2)) << 4);
#pragma unroll
    for (int u = 0; u < 4; ++u) vx[u] = ((lw * 4 + u) * 8 + r8) * 1280 + ((pos ^ (4 * (u & 1) + (r8 >> 1))) << 4);
    u32 slot = 0;
    auto issue1 = [&]() {
      const u32 lb = slot + lw * 6144, lx = slot + 24576 + lw * 4096;
      DMAP(POL_W, vw[0], wq, lb, 0); DMAP(POL_W, vw[1], wq, lb, 1024); DMAP(POL_W, vw[2], wq, lb, 2048);
      DMAP(POL_X, vx[0], xg, lx, 0); DMAP(POL_X, vx[1], xg, lx, 1024);
      DMAP(POL_W, vw[3], wq, lb, 3072); DMAP(POL_W, vw[4], wq, lb, 4096); DMAP(POL_W, vw[5], wq, lb, 5120);
      DMAP(POL_X, vx[2], xg, lx, 2048); DMAP(POL_X, vx[3], xg, lx, 3072);
      wq += 64 * 12288; xg += 128;
      slot = slot == 2 * SLOT ? 0 : slot + SLOT;
    };
    issue1(); issue1();
    u32 vk[4], vo[5];
#pragma unroll
    for (int u = 0; u < 4; ++u) vk[u] = (u * 8 + r8) * 384 + ((pos ^ (4 * (u & 1) + (r8 >> 1))) << 4);
#pragma unroll
    for (int u = 0; u < 5; ++u) { const u32 id = u * 1024 + lane * 16; vo[u] = (id / 320) * 1280 + id % 320; }
    auto issue2 = [&]() {
      const u32 lv = slot + lw * 4096, lo = slot + 16384 + lw * 5120;
      DMAP(POL_V, vk[0], vg, lv, 0); DMAP(POL_V, vk[1], vg, lv, 1024);
      DMAP(POL_O, vo[0], wo, lo, 0); DMAP(POL_O, vo[1], wo, lo, 1024); DMAP(POL_O, vo[2], wo, lo, 2048);
      DMAP(POL_V, vk[2], vg, lv, 2048); DMAP(POL_V, vk[3], vg, lv, 3072);
      DMAP(POL_O, vo[3], wo, lo, 3072); DMAP(POL_O, vo[4], wo, lo, 4096);
      wo += 64 * 1280;
      slot = slot == 2 * SLOT ? 0 : slot + SLOT;
    };
#pragma unroll 1
    for (int s = 0; s < 8; ++s) { STAMP(32 + s); BAR_L(10); STAMP(8 + s); issue1(); }
    STAMP(40); BAR_L(10); STAMP(16);
    {
      const u32 lk = slot + lw * 12288;
#pragma unroll
      for (int cb = 0; cb < 3; ++cb) {
        const char* kk = kg + cb * 128;
        DMAP(POL_K, vk[0], kk, lk, cb * 4096); DMAP(POL_K, vk[1], kk, lk, cb * 4096 + 1024);
        DMAP(POL_K, vk[2], kk, lk, cb * 4096 + 2048); DMAP(POL_K, vk[3], kk, lk, cb * 4096 + 3072);
      }
      slot = slot == 2 * SLOT ? 0 : slot + SLOT;
    }
    STAMP(41); BAR_L(12); STAMP(17); issue2(); vg += 128;
    int third = 1;
#pragma unroll 1
    for (int s = 10; s < 21; ++s) {
      STAMP(32 + s); BAR_L(9); STAMP(8 + s); issue2();
      vg += third == 2 ? 12288 - 256 : 128; third = third == 2 ? 0 : third + 1;
    }
    STAMP(53); BAR_L(9); STAMP(29);
    BAR_L(0); STAMP(30);
    STAMP(1);
  } else {
    const int w = wid;
    const u32 hf = lane >> 5, blk = (lane >> 4) & 1, q4 = (lane & 15) >> 2, p4 = lane & 3, r32 = lane & 31;
    const u32 colp = 32 * blk + 16 * (p4 & 1) + 8 * (p4 >> 1);
    const u32 wl0 = hf * 3072 + q4 * 128 + ((q4 >> 1) * 64) + colp;
    const u32 wl1 = wl0 ^ 64;
    const u32 rowl = r32 * 128 + ((hf ^ ((r32 >> 1) & 7)) << 4);
    u32 slot = 0;
    f32x16 q[6];
#pragma unroll
    for (int c = 0; c < 6; ++c) q[c] = f32x16{};
    bf16x8 fa[2][6], fb[2];
#pragma unroll
    for (int c = 0; c < 6; ++c) fa[1][c] = bf16x8{};
    fb[1] = bf16x8{};
#pragma unroll 1
    for (int s = 0; s < 10; ++s) {
      u32 a0 = slot + wl0, a1 = slot + wl1, xa = slot + 24576 + w * 4096 + rowl;
      OPAQUE(a0); OPAQUE(a1); OPAQUE(xa);
      BAR_C();
#pragma unroll
      for (int ks = 0; ks < 4; ++ks) {
        fb[ks & 1] = lds_row(xa ^ (ks << 5));
#pragma unroll
        for (int c = 0; c < 6; ++c) {
          const u32 ad = ((c & 1) ? a1 : a0) + ks * 6144 + (c >> 1) * 1024;
          fa[ks & 1][c] = lds_tr(ad, ad + 512);
        }
#pragma unroll
        for (int c = 0; c < 6; ++c) q[c] = MFMA(fa[(ks & 1) ^ 1][c], fb[(ks & 1) ^ 1], q[c]);
        SGB(0x100, 3); SGB(0x8, 1);
#pragma unroll
        for (int c = 1; c < 6; ++c) { SGB(0x100, 2); SGB(0x8, 1); }
        __builtin_amdgcn_sched_barrier(0);
      }
      slot = slot == 2 * SLOT ? 0 : slot + SLOT;
    }
    STAMP(1);
    BAR_C();
    bf16x8 pf[2];
    {
      f32x16 sc = f32x16{};
      const u32 ka = slot + w * 12288 + rowl;
      bf16x8 kf[12];
#pragma unroll
      for (int i = 0; i < 12; ++i) kf[i] = lds_row((ka + (i >> 2) * 4096) ^ ((i & 3) << 5));
#pragma unroll
      for (int c = 0; c < 6; ++c) q[c] = MFMA(fa[1][c], fb[1], q[c]);
      __builtin_amdgcn_sched_barrier(0);
#pragma unroll
      for (int c = 0; c < 6; ++c)
#pragma unroll
        for (int s = 0; s < 2; ++s) sc = MFMA(kf[2 * c + s], pack8(q[c], s), sc);
      slot = slot == 2 * SLOT ? 0 : slot + SLOT;
      float m = sc[0];
#pragma unroll
      for (int i = 1; i < 16; ++i) m = fmaxf(m, sc[i]);
      { auto r = __builtin_amdgcn_permlane32_swap(__float_as_uint(m), __float_as_uint(m), false, false);
        m = fmaxf(__uint_as_float(r[0]), __uint_as_float(r[1])); }
      const float cs = 0.07216878364870322f * 1.4426950408889634f;
      float sum = 0.f;
#pragma unroll
      for (int i = 0; i < 16; ++i) { sc[i] = __builtin_amdgcn_exp2f((sc[i] - m) * cs); sum += sc[i]; }
      { auto r = __builtin_amdgcn_permlane32_swap(__float_as_uint(sum), __float_as_uint(sum), false, false);
        sum = __uint_as_float(r[0]) + __uint_as_float(r[1]); }
      const float inv = 1.0f / sum;
#pragma unroll
      for (int i = 0; i < 16; ++i) sc[i] *= inv;
      const u32 po = WS_P + ((br * 16 + rg * 4 + w) * 32 + h) * 2048 + lane * 16;
      __builtin_amdgcn_raw_buffer_store_b128(__builtin_bit_cast(u32x4, pack8(sc, 0)), wsr, po, 0, 16);
      __builtin_amdgcn_raw_buffer_store_b128(__builtin_bit_cast(u32x4, pack8(sc, 1)), wsr, po + 1024, 0, 16);
    }
    const u64 sig = mix64(gv);
    const u64 want = ((u64)__builtin_amdgcn_readlane((u32)(sig >> 32), 8) << 32) | (u32)__builtin_amdgcn_readlane((u32)sig, 8);
    asm volatile("s_waitcnt vmcnt(0)" ::: "memory");
    if (lane < 4)
      __hip_atomic_store((GAS u64*)(a.ws + WS_FP + (br * 128 + (hg * 4 + lane) * 4 + rg) * 128 + (ct * 4 + w) * 8), sig,
                         __ATOMIC_RELAXED, __HIP_MEMORY_SCOPE_AGENT);
    STAMP(2);

    f32x16 o[5], t[5];
#pragma unroll
    for (int c = 0; c < 5; ++c) o[c] = f32x16{};
    const u32 ol = (8 * hf + q4) * 320 + colp + 16384;
    bf16x8 gb[2][5], ga[2];
#pragma unroll 1
    for (int hx = 0; hx < 4; ++hx) {
#pragma unroll
      for (int st = 0; st < 3; ++st) {
        u32 va = slot + w * 4096 + rowl, oa = slot + ol;
        OPAQUE(va); OPAQUE(oa);
        BAR_C();
        if (st == 2) {
          if (hx == 0) {
            const GAS u64* fp = (const GAS u64*)(a.ws + WS_FP + self * 128 + ((lane & 3) * 4 + w) * 8);
#pragma unroll 1
            for (u32 spin = 0; spin < SPIN_CAP; ++spin) {
              const u64 f = __hip_atomic_load(fp, __ATOMIC_RELAXED, __HIP_MEMORY_SCOPE_AGENT);
              if (__all(f == want)) break;
              __builtin_amdgcn_s_sleep(2);
            }
            __builtin_amdgcn_fence(__ATOMIC_ACQUIRE, "wavefront");
          }
          const u32 po = WS_P + ((br * 16 + rg * 4 + w) * 32 + hg * 4 + hx) * 2048 + lane * 16;
          pf[0] = __builtin_bit_cast(bf16x8, __builtin_amdgcn_raw_buffer_load_b128(wsr, po, 0, 16));
          pf[1] = __builtin_bit_cast(bf16x8, __builtin_amdgcn_raw_buffer_load_b128(wsr, po + 1024, 0, 16));
          __builtin_amdgcn_sched_barrier(0);
        }
#pragma unroll
        for (int ks = 0; ks < 4; ++ks) {
          ga[ks & 1] = lds_row(va ^ (ks << 5));
#pragma unroll
          for (int c = 0; c < 5; ++c) {
            const u32 ad = oa + ks * 5120 + c * 64;
            gb[ks & 1][c] = lds_tr(ad, ad + 1280);
          }
          if (st == 0 && ks == 0) {
            __builtin_amdgcn_sched_barrier(0);
            if (hx) {
#pragma unroll
              for (int c = 0; c < 5; ++c) t[c] = MFMA(ga[1], gb[1][c], t[c]);
#pragma unroll
              for (int c = 0; c < 5; ++c)
#pragma unroll
                for (int s = 0; s < 2; ++s) o[c] = MFMA(pack8(t[c], s), pf[s], o[c]);
            }
          } else {
#pragma unroll
            for (int c = 0; c < 5; ++c)
              t[c] = MFMA(ga[(ks & 1) ^ 1], gb[(ks & 1) ^ 1][c], (st == 0 && ks == 1) ? f32x16{} : t[c]);
            SGB(0x100, 3); SGB(0x8, 1);
#pragma unroll
            for (int c = 1; c < 5; ++c) { SGB(0x100, 2); SGB(0x8, 1); }
          }
          __builtin_amdgcn_sched_barrier(0);
        }
        slot = slot == 2 * SLOT ? 0 : slot + SLOT;
      }
    }
#pragma unroll
    for (int c = 0; c < 5; ++c) t[c] = MFMA(ga[1], gb[1][c], t[c]);
#pragma unroll
    for (int c = 0; c < 5; ++c)
#pragma unroll
      for (int s = 0; s < 2; ++s) o[c] = MFMA(pack8(t[c], s), pf[s], o[c]);
    STAMP(3);
    {
      const u32 owner = br * 128 + ((2 * w + blk) * 4 + ct) * 4 + rg;
      const u32 io = WS_IN + (owner * 8 + hg) * 5120 + (hf * 16 + (lane & 15)) * 16;
#pragma unroll
      for (int c = 0; c < 5; ++c)
#pragma unroll
        for (int s = 0; s < 2; ++s)
          __builtin_amdgcn_raw_buffer_store_b128(__builtin_bit_cast(u32x4, pack8(o[c], s)), wsr, io + (2 * c + s) * 512, 0, 16);
      asm volatile("s_waitcnt vmcnt(0)" ::: "memory");
      if (lane == 4 || lane == 5)
        __hip_atomic_store((GAS u64*)(a.ws + WS_FR + (br * 128 + ((2 * w + lane - 4) * 4 + ct) * 4 + rg) * 128 + hg * 8), sig,
                           __ATOMIC_RELAXED, __HIP_MEMORY_SCOPE_AGENT);
    }
    STAMP(4);
  }

  if (wid >= 3) {
    const int c = wid - 3;
    const u64 sig = mix64(gv);
    const u64 want = ((u64)__builtin_amdgcn_readlane((u32)(sig >> 32), 8) << 32) | (u32)__builtin_amdgcn_readlane((u32)sig, 8);
    const GAS u64* fr = (const GAS u64*)(a.ws + WS_FR + self * 128 + (lane & 7) * 8);
#pragma unroll 1
    for (u32 spin = 0; spin < SPIN_CAP; ++spin) {
      const u64 f = __hip_atomic_load(fr, __ATOMIC_RELAXED, __HIP_MEMORY_SCOPE_AGENT);
      if (__all(f == want)) break;
      __builtin_amdgcn_s_sleep(2);
    }
    __builtin_amdgcn_fence(__ATOMIC_ACQUIRE, "wavefront");
    STAMP(5);
    const u32 io = WS_IN + self * 8 * 5120 + c * 1024 + lane * 16;
    u32x4 v[8];
#pragma unroll
    for (int g = 0; g < 8; ++g) v[g] = __builtin_amdgcn_raw_buffer_load_b128(wsr, io + g * 5120, 0, 16);
    float acc[8];
#pragma unroll
    for (int j = 0; j < 8; ++j) acc[j] = 0.f;
#pragma unroll
    for (int g = 0; g < 8; ++g)
#pragma unroll
      for (int j = 0; j < 4; ++j) {
        acc[2 * j] += __uint_as_float(v[g][j] << 16);
        acc[2 * j + 1] += __uint_as_float(v[g][j] & 0xffff0000u);
      }
    const u32x4 r = {pk2(acc[0], acc[1]), pk2(acc[2], acc[3]), pk2(acc[4], acc[5]), pk2(acc[6], acc[7])};
    const int row = rg * 128 + hg * 16 + (lane & 15);
    const int col = br * 640 + ct * 160 + c * 32 + (lane >> 5) * 16 + ((lane >> 4) & 1) * 8;
    *(u32x4*)(a.out + row * 1280 + col) = r;
    if (wid == 4 && lane == 8)
      __hip_atomic_store((GAS u64*)(a.ws + WS_G + self * 128), sig, __ATOMIC_RELAXED, __HIP_MEMORY_SCOPE_AGENT);
    STAMP(6);
  }
  STAMP_DUMP();
}

extern "C" void kernel_launch(void* const* d_in, const int* in_sizes, int n_in, void* d_out, int out_size,
                              void* d_ws, size_t ws_size, hipStream_t stream) {
  Args a;
  for (int i = 0; i < 10; ++i) a.in[i] = (const u16*)d_in[i];
  a.out = (u16*)d_out;
  a.ws = (char*)d_ws;
  (void)hipFuncSetAttribute((const void*)fused_attn_block, hipFuncAttributeMaxDynamicSharedMemorySize, LDS_BYTES);
  fused_attn_block<<<256, 512, LDS_BYTES, stream>>>(a);
}
```

```cpp
#include <hip/hip_runtime.h>

struct Args { const void* in[10]; void* out; void* ws; };
constexpr unsigned LDS_BYTES = 4 * 40960;

__global__ __launch_bounds__(512, 2) void fused_attn_block(Args a) {
  asm volatile(
    "s_bfe_i32 s38, %[bid], 0x10002\n\t"
    "s_and_b32 s38, s38, 40\n\t"
    "s_load_dwordx8 s[8:15], %[ka], s38\n\t"
    "s_load_dwordx2 s[16:17], %[ka], s38 offset:32\n\t"
    "s_load_dwordx4 s[20:23], %[ka], 0x50\n\t"
    "v_readfirstlane_b32 s37, %[tid]\n\t"
    "v_and_b32 v251, 63, %[tid]\n\t"
    "s_lshr_b32 s28, %[bid], 5\n\t"
    "s_bfe_u32 s29, %[bid], 0x20003\n\t"
    "s_and_b32 s30, %[bid], 3\n\t"
    "s_lshl_b32 s30, s30, 3\n\t"
    "s_add_u32 s30, s30, s28\n\t"
    "s_add_u32 s31, s29, s28\n\t"
    "s_and_b32 s31, s31, 3\n\t"
    "s_bfe_u32 s18, s37, 0x20006\n\t"
    "s_cmp_lt_u32 s37, 256\n\t"
    "s_cbranch_scc1 CONS%=\n\t"
    "v_lshrrev_b32 v20, 3, v251\n\t"
    "v_and_b32 v21, 7, v251\n\t"
    "v_lshrrev_b32 v22, 2, v251\n\t"
    "v_and_b32 v22, 4, v22\n\t"
    "v_xor_b32 v22, v21, v22\n\t"
    "v_mul_u32_u24 v23, 0x3000, v20\n\t"
    "v_lshl_or_b32 v1, v22, 4, v23\n\t"
    "s_mul_i32 s38, s30, 384\n\t"
    "s_mul_i32 s39, s18, 196608\n\t"
    "s_add_u32 s38, s38, s39\n\t"
    "v_xor_b32 v2, 32, v1\n\t"
    "v_add_u32 v2, 0x18000, v2\n\t"
    "s_lshl_b32 s39, s29, 2\n\t"
    "s_add_u32 s39, s39, s18\n\t"
    "s_mul_i32 s39, s39, 40960\n\t"
    "v_lshrrev_b32 v24, 4, v251\n\t"
    "v_xor_b32 v24, v21, v24\n\t"
    "v_lshlrev_b32 v24, 4, v24\n\t"
    "v_mul_u32_u24 v23, 0x500, v20\n\t"
    "v_or_b32 v3, v24, v23\n\t"
    "s_lshr_b32 s69, s31, 1\n\t"
    "s_mul_i32 s68, s69, 0xc0000\n\t"
    "s_lshl_b32 s69, s69, 7\n\t"
    "v_xor_b32 v4, 64, v3\n\t"
    "v_add_u32 v4, 0x2800, v4\n\t"
    "v_add_u32 v5, 0x5000, v3\n\t"
    "v_add_u32 v6, 0x5000, v4\n\t"
    "s_mul_i32 s56, s18, 6144\n\t"
    "s_lshl_b32 s63, s18, 12\n\t"
    "s_add_u32 s57, s63, 24576\n\t"
    "s_waitcnt lgkmcnt(0)\n\t"
    "s_add_u32 s40, s14, s38\n\t"
    "s_addc_u32 s41, s15, 0\n\t"
    "s_add_u32 s44, s40, s68\n\t"
    "s_addc_u32 s45, s41, 0\n\t"
    "s_add_u32 m0, s56, 0\n\t"
    "s_nop 0\n\t"
    "global_load_lds_dwordx4 v1, s[44:45]\n\t"
    "s_add_u32 m0, s56, 896\n\t"
    "s_nop 0\n\t"
    "global_load_lds_dwordx4 v1, s[44:45] offset:128\n\t"
    "s_add_u32 m0, s56, 1792\n\t"
    "s_nop 0\n\t"
    "global_load_lds_dwordx4 v1, s[44:45] offset:256\n\t"
    "s_add_u32 s42, s8, s39\n\t"
    "s_addc_u32 s43, s9, 0\n\t"
    "s_add_u32 s46, s42, s69\n\t"
    "s_addc_u32 s47, s43, 0\n\t"
    "s_add_u32 m0, s57, 0\n\t"
    "s_nop 0\n\t"
    "global_load_lds_dwordx4 v3, s[46:47]\n\t"
    "s_add_u32 m0, s57, 1024\n\t"
    "s_nop 0\n\t"
    "global_load_lds_dwordx4 v4, s[46:47]\n\t"
    "s_add_u32 m0, s56, 3072\n\t"
    "s_nop 0\n\t"
    "global_load_lds_dwordx4 v2, s[44:45]\n\t"
    "s_add_u32 m0, s56, 3968\n\t"
    "s_nop 0\n\t"
    "global_load_lds_dwordx4 v2, s[44:45] offset:128\n\t"
    "s_add_u32 m0, s56, 4864\n\t"
    "s_nop 0\n\t"
    "global_load_lds_dwordx4 v2, s[44:45] offset:256\n\t"
    "s_add_u32 m0, s57, 2048\n\t"
    "s_nop 0\n\t"
    "global_load_lds_dwordx4 v5, s[46:47]\n\t"
    "s_add_u32 m0, s57, 3072\n\t"
    "s_nop 0\n\t"
    "global_load_lds_dwordx4 v6, s[46:47]\n\t"
    "s_mov_b32 s64, 0x86542703\n\t"
    "s_mov_b32 s65, 0x9\n\t"
    "s_mov_b32 s60, 0x5\n\t"
    "s_mov_b32 s61, 0x9\n\t"
    "s_cmp_eq_u32 s31, 0\n\t"
    "s_cselect_b32 s64, 0x76853241, s64\n\t"
    "s_cselect_b32 s65, 0x9, s65\n\t"
    "s_cselect_b32 s60, 0x2, s60\n\t"
    "s_cselect_b32 s61, 0x6, s61\n\t"
    "s_cmp_eq_u32 s31, 1\n\t"
    "s_cselect_b32 s64, 0x87543612, s64\n\t"
    "s_cselect_b32 s65, 0x9, s65\n\t"
    "s_cselect_b32 s60, 0x4, s60\n\t"
    "s_cselect_b32 s61, 0x8, s61\n\t"
    "s_cmp_eq_u32 s31, 2\n\t"
    "s_cselect_b32 s64, 0x79643250, s64\n\t"
    "s_cselect_b32 s65, 0x8, s65\n\t"
    "s_cselect_b32 s60, 0x3, s60\n\t"
    "s_cselect_b32 s61, 0x7, s61\n\t"
    "s_mov_b32 s58, 0\n\t"
    "s_and_b32 s38, s64, 15\n\t"
    "s_lshr_b64 s[64:65], s[64:65], 4\n\t"
    "s_mul_i32 s39, s38, 0xc0000\n\t"
    "s_add_u32 s44, s40, s39\n\t"
    "s_addc_u32 s45, s41, 0\n\t"
    "s_lshl_b32 s38, s38, 7\n\t"
    "s_add_u32 s46, s42, s38\n\t"
    "s_addc_u32 s47, s43, 0\n\t"
    "s_add_u32 s58, s58, 40960\n\t"
    "s_cmp_eq_u32 s58, 122880\n\t"
    "s_cselect_b32 s58, 0, s58\n\t"
    "s_add_u32 s54, s56, s58\n\t"
    "s_add_u32 s55, s57, s58\n\t"
    "s_add_u32 m0, s54, 0\n\t"
    "s_nop 0\n\t"
    "global_load_lds_dwordx4 v1, s[44:45]\n\t"
    "s_add_u32 m0, s54, 896\n\t"
    "s_nop 0\n\t"
    "global_load_lds_dwordx4 v1, s[44:45] offset:128\n\t"
    "s_add_u32 m0, s54, 1792\n\t"
    "s_nop 0\n\t"
    "global_load_lds_dwordx4 v1, s[44:45] offset:256\n\t"
    "s_add_u32 m0, s55, 0\n\t"
    "s_nop 0\n\t"
    "global_load_lds_dwordx4 v3, s[46:47]\n\t"
    "s_add_u32 m0, s55, 1024\n\t"
    "s_nop 0\n\t"
    "global_load_lds_dwordx4 v4, s[46:47]\n\t"
    "s_bfe_u32 s19, %[bid], 0x10002\n\t"
    "s_lshr_b32 s34, s30, 2\n\t"
    "s_and_b32 s35, s30, 3\n\t"
    "s_lshl_b32 s36, s19, 5\n\t"
    "s_add_u32 s36, s36, s30\n\t"
    "s_lshl_b32 s36, s36, 2\n\t"
    "s_add_u32 s36, s36, s29\n\t"
    "s_mov_b32 s24, s22\n\t"
    "s_and_b32 s25, s23, 0xffff\n\t"
    "s_mov_b32 s26, 0xe10000\n\t"
    "s_mov_b32 s27, 0x20000\n\t"
    "s_lshl_b32 s38, s36, 7\n\t"
    "v_mov_b32 v16, 0\n\t"
    "buffer_load_dwordx2 v[18:19], v16, s[24:27], s38 offen sc1\n\t"
    "v_mul_u32_u24 v23, 0x180, v20\n\t"
    "v_or_b32 v7, v24, v23\n\t"
    "v_xor_b32 v8, 64, v7\n\t"
    "v_add_u32 v8, 0xc00, v8\n\t"
    "v_add_u32 v9, 0x1800, v7\n\t"
    "v_add_u32 v10, 0x1800, v8\n\t"
    "v_add_u32 v25, 0, v251\n\t"
    "v_mul_u32_u24 v26, 0xccd, v25\n\t"
    "v_lshrrev_b32 v26, 16, v26\n\t"
    "v_mul_u32_u24 v27, 20, v26\n\t"
    "v_sub_u32 v27, v25, v27\n\t"
    "v_lshrrev_b32 v28, 2, v26\n\t"
    "v_and_b32 v28, 2, v28\n\t"
    "v_xor_b32 v27, v27, v28\n\t"
    "v_mul_u32_u24 v26, 0x500, v26\n\t"
    "v_lshl_add_u32 v11, v27, 4, v26\n\t"
    "v_add_u32 v25, 64, v251\n\t"
    "v_mul_u32_u24 v26, 0xccd, v25\n\t"
    "v_lshrrev_b32 v26, 16, v26\n\t"
    "v_mul_u32_u24 v27, 20, v26\n\t"
    "v_sub_u32 v27, v25, v27\n\t"
    "v_lshrrev_b32 v28, 2, v26\n\t"
    "v_and_b32 v28, 2, v28\n\t"
    "v_xor_b32 v27, v27, v28\n\t"
    "v_mul_u32_u24 v26, 0x500, v26\n\t"
    "v_lshl_add_u32 v12, v27, 4, v26\n\t"
    "v_add_u32 v25, 128, v251\n\t"
    "v_mul_u32_u24 v26, 0xccd, v25\n\t"
    "v_lshrrev_b32 v26, 16, v26\n\t"
    "v_mul_u32_u24 v27, 20, v26\n\t"
    "v_sub_u32 v27, v25, v27\n\t"
    "v_lshrrev_b32 v28, 2, v26\n\t"
    "v_and_b32 v28, 2, v28\n\t"
    "v_xor_b32 v27, v27, v28\n\t"
    "v_mul_u32_u24 v26, 0x500, v26\n\t"
    "v_lshl_add_u32 v13, v27, 4, v26\n\t"
    "v_add_u32 v25, 192, v251\n\t"
    "v_mul_u32_u24 v26, 0xccd, v25\n\t"
    "v_lshrrev_b32 v26, 16, v26\n\t"
    "v_mul_u32_u24 v27, 20, v26\n\t"
    "v_sub_u32 v27, v25, v27\n\t"
    "v_lshrrev_b32 v28, 2, v26\n\t"
    "v_and_b32 v28, 2, v28\n\t"
    "v_xor_b32 v27, v27, v28\n\t"
    "v_mul_u32_u24 v26, 0x500, v26\n\t"
    "v_lshl_add_u32 v14, v27, 4, v26\n\t"
    "v_add_u32 v25, 256, v251\n\t"
    "v_mul_u32_u24 v26, 0xccd, v25\n\t"
    "v_lshrrev_b32 v26, 16, v26\n\t"
    "v_mul_u32_u24 v27, 20, v26\n\t"
    "v_sub_u32 v27, v25, v27\n\t"
    "v_lshrrev_b32 v28, 2, v26\n\t"
    "v_and_b32 v28, 2, v28\n\t"
    "v_xor_b32 v27, v27, v28\n\t"
    "v_mul_u32_u24 v26, 0x500, v26\n\t"
    "v_lshl_add_u32 v15, v27, 4, v26\n\t"
    "s_lshl_b32 s38, s29, 2\n\t"
    "s_add_u32 s38, s38, s18\n\t"
    "s_lshl_b32 s38, s38, 5\n\t"
    "s_add_u32 s39, s38, s30\n\t"
    "s_mul_i32 s39, s39, 12288\n\t"
    "s_add_u32 s48, s10, s39\n\t"
    "s_addc_u32 s49, s11, 0\n\t"
    "s_and_b32 s39, s30, 28\n\t"
    "s_add_u32 s39, s39, s38\n\t"
    "s_mul_i32 s39, s39, 12288\n\t"
    "s_add_u32 s50, s12, s39\n\t"
    "s_addc_u32 s51, s13, 0\n\t"
    "s_mul_i32 s38, s34, 48\n\t"
    "s_add_u32 s38, s38, s18\n\t"
    "s_mul_i32 s38, s38, 20480\n\t"
    "s_mul_i32 s39, s35, 320\n\t"
    "s_add_u32 s38, s38, s39\n\t"
    "s_add_u32 s52, s16, s38\n\t"
    "s_addc_u32 s53, s17, 0\n\t"
    "s_mov_b32 s66, 0x65742130\n\t"
    "s_mov_b32 s67, 0xa9b8\n\t"
    "s_mov_b32 s62, 0x2db1c9\n\t"
    "s_cmp_eq_u32 s31, 0\n\t"
    "s_cselect_b32 s66, 0x68532410, s66\n\t"
    "s_cselect_b32 s67, 0xba97, s67\n\t"
    "s_cselect_b32 s62, 0x1879c9, s62\n\t"
    "s_cmp_eq_u32 s31, 1\n\t"
    "s_cselect_b32 s66, 0x96435201, s66\n\t"
    "s_cselect_b32 s67, 0xba87, s67\n\t"
    "s_cselect_b32 s62, 0x4d86c9, s62\n\t"
    "s_cmp_eq_u32 s31, 2\n\t"
    "s_cselect_b32 s66, 0x75463102, s66\n\t"
    "s_cselect_b32 s67, 0xb98a, s67\n\t"
    "s_cselect_b32 s62, 0xe4d2c9, s62\n\t"
    "s_add_u32 s70, s63, 122880\n\t"
    "s_mov_b32 s59, 2\n\t"
    "s_waitcnt vmcnt(6)\n\t"
    "s_barrier\n\t"
    "s_add_u32 m0, s54, 3072\n\t"
    "s_nop 0\n\t"
    "global_load_lds_dwordx4 v2, s[44:45]\n\t"
    "s_add_u32 m0, s54, 3968\n\t"
    "s_nop 0\n\t"
    "global_load_lds_dwordx4 v2, s[44:45] offset:128\n\t"
    "s_add_u32 m0, s54, 4864\n\t"
    "s_nop 0\n\t"
    "global_load_lds_dwordx4 v2, s[44:45] offset:256\n\t"
    "s_add_u32 m0, s55, 2048\n\t"
    "s_nop 0\n\t"
    "global_load_lds_dwordx4 v5, s[46:47]\n\t"
    "s_add_u32 m0, s55, 3072\n\t"
    "s_nop 0\n\t"
    "global_load_lds_dwordx4 v6, s[46:47]\n\t"
    "s_and_b32 s38, s64, 15\n\t"
    "s_lshr_b64 s[64:65], s[64:65], 4\n\t"
    "s_mul_i32 s39, s38, 0xc0000\n\t"
    "s_add_u32 s44, s40, s39\n\t"
    "s_addc_u32 s45, s41, 0\n\t"
    "s_lshl_b32 s38, s38, 7\n\t"
    "s_add_u32 s46, s42, s38\n\t"
    "s_addc_u32 s47, s43, 0\n\t"
    "s_add_u32 s58, s58, 40960\n\t"
    "s_cmp_eq_u32 s58, 122880\n\t"
    "s_cselect_b32 s58, 0, s58\n\t"
    "s_add_u32 s54, s56, s58\n\t"
    "s_add_u32 s55, s57, s58\n\t"
    "s_branch LP1S%=\n\t"
    "LP1%=:\n\t"
    "s_barrier\n\t"
    "LP1S%=:\n\t"
    "s_add_u32 m0, s54, 0\n\t"
    "s_nop 0\n\t"
    "global_load_lds_dwordx4 v1, s[44:45]\n\t"
    "s_add_u32 m0, s54, 896\n\t"
    "s_nop 0\n\t"
    "global_load_lds_dwordx4 v1, s[44:45] offset:128\n\t"
    "s_add_u32 m0, s54, 1792\n\t"
    "s_nop 0\n\t"
    "global_load_lds_dwordx4 v1, s[44:45] offset:256\n\t"
    "s_add_u32 m0, s55, 0\n\t"
    "s_nop 0\n\t"
    "global_load_lds_dwordx4 v3, s[46:47]\n\t"
    "s_add_u32 m0, s55, 1024\n\t"
    "s_nop 0\n\t"
    "global_load_lds_dwordx4 v4, s[46:47]\n\t"
    "s_add_u32 m0, s54, 3072\n\t"
    "s_nop 0\n\t"
    "global_load_lds_dwordx4 v2, s[44:45]\n\t"
    "s_add_u32 m0, s54, 3968\n\t"
    "s_nop 0\n\t"
    "global_load_lds_dwordx4 v2, s[44:45] offset:128\n\t"
    "s_add_u32 m0, s54, 4864\n\t"
    "s_nop 0\n\t"
    "global_load_lds_dwordx4 v2, s[44:45] offset:256\n\t"
    "s_add_u32 m0, s55, 2048\n\t"
    "s_nop 0\n\t"
    "global_load_lds_dwordx4 v5, s[46:47]\n\t"
    "s_add_u32 m0, s55, 3072\n\t"
    "s_nop 0\n\t"
    "global_load_lds_dwordx4 v6, s[46:47]\n\t"
    "NT1B%=:\n\t"
    "s_cmp_eq_u32 s59, s60\n\t"
    "s_cbranch_scc1 LK0%=\n\t"
    "s_cmp_eq_u32 s59, s61\n\t"
    "s_cbranch_scc1 LK12%=\n\t"
    "s_and_b32 s38, s64, 15\n\t"
    "s_lshr_b64 s[64:65], s[64:65], 4\n\t"
    "s_mul_i32 s39, s38, 0xc0000\n\t"
    "s_add_u32 s44, s40, s39\n\t"
    "s_addc_u32 s45, s41, 0\n\t"
    "s_lshl_b32 s38, s38, 7\n\t"
    "s_add_u32 s46, s42, s38\n\t"
    "s_addc_u32 s47, s43, 0\n\t"
    "s_add_u32 s58, s58, 40960\n\t"
    "s_cmp_eq_u32 s58, 122880\n\t"
    "s_cselect_b32 s58, 0, s58\n\t"
    "s_add_u32 s54, s56, s58\n\t"
    "s_add_u32 s55, s57, s58\n\t"
    "s_waitcnt vmcnt(10)\n\t"
    "LP1B%=:\n\t"
    "s_add_u32 s59, s59, 1\n\t"
    "s_cmp_lt_u32 s59, 10\n\t"
    "s_cbranch_scc1 LP1%=\n\t"
    "s_mul_i32 s56, s18, 5120\n\t"
    "s_add_u32 s56, s56, 16384\n\t"
    "s_and_b32 s38, s66, 15\n\t"
    "s_lshr_b64 s[66:67], s[66:67], 4\n\t"
    "s_mul_i32 s39, s38, 81920\n\t"
    "s_add_u32 s44, s52, s39\n\t"
    "s_addc_u32 s45, s53, 0\n\t"
    "s_mul_i32 s39, s38, 11\n\t"
    "s_lshr_b32 s39, s39, 5\n\t"
    "s_mul_i32 s39, s39, 11904\n\t"
    "s_lshl_b32 s38, s38, 7\n\t"
    "s_add_u32 s38, s38, s39\n\t"
    "s_add_u32 s46, s50, s38\n\t"
    "s_addc_u32 s47, s51, 0\n\t"
    "s_and_b32 s38, s62, 3\n\t"
    "s_lshr_b32 s62, s62, 2\n\t"
    "s_mul_i32 s38, s38, 40960\n\t"
    "s_add_u32 s55, s38, s63\n\t"
    "s_add_u32 s54, s38, s56\n\t"
    "s_mov_b32 s78, 11\n\t"
    "LP2%=:\n\t"
    "s_barrier\n\t"
    "LP2S%=:\n\t"
    "s_add_u32 m0, s55, 0\n\t"
    "s_nop 0\n\t"
    "global_load_lds_dwordx4 v7, s[46:47]\n\t"
    "s_add_u32 m0, s55, 1024\n\t"
    "s_nop 0\n\t"
    "global_load_lds_dwordx4 v8, s[46:47]\n\t"
    "s_add_u32 m0, s54, 0\n\t"
    "s_nop 0\n\t"
    "global_load_lds_dwordx4 v11, s[44:45]\n\t"
    "s_add_u32 m0, s54, 1024\n\t"
    "s_nop 0\n\t"
    "global_load_lds_dwordx4 v12, s[44:45]\n\t"
    "s_add_u32 m0, s54, 2048\n\t"
    "s_nop 0\n\t"
    "global_load_lds_dwordx4 v13, s[44:45]\n\t"
    "s_add_u32 m0, s55, 2048\n\t"
    "s_nop 0\n\t"
    "global_load_lds_dwordx4 v9, s[46:47]\n\t"
    "s_add_u32 m0, s55, 3072\n\t"
    "s_nop 0\n\t"
    "global_load_lds_dwordx4 v10, s[46:47]\n\t"
    "s_add_u32 m0, s54, 3072\n\t"
    "s_nop 0\n\t"
    "global_load_lds_dwordx4 v14, s[44:45]\n\t"
    "s_add_u32 m0, s54, 4096\n\t"
    "s_nop 0\n\t"
    "global_load_lds_dwordx4 v15, s[44:45]\n\t"
    "NT2B%=:\n\t"
    "s_and_b32 s38, s66, 15\n\t"
    "s_lshr_b64 s[66:67], s[66:67], 4\n\t"
    "s_mul_i32 s39, s38, 81920\n\t"
    "s_add_u32 s44, s52, s39\n\t"
    "s_addc_u32 s45, s53, 0\n\t"
    "s_mul_i32 s39, s38, 11\n\t"
    "s_lshr_b32 s39, s39, 5\n\t"
    "s_mul_i32 s39, s39, 11904\n\t"
    "s_lshl_b32 s38, s38, 7\n\t"
    "s_add_u32 s38, s38, s39\n\t"
    "s_add_u32 s46, s50, s38\n\t"
    "s_addc_u32 s47, s51, 0\n\t"
    "s_and_b32 s38, s62, 3\n\t"
    "s_lshr_b32 s62, s62, 2\n\t"
    "s_mul_i32 s38, s38, 40960\n\t"
    "s_add_u32 s55, s38, s63\n\t"
    "s_add_u32 s54, s38, s56\n\t"
    "s_add_u32 s59, s59, 1\n\t"
    "s_cmp_eq_u32 s59, s78\n\t"
    "s_cbranch_scc1 LEX%=\n\t"
    "s_cmp_lt_u32 s59, 22\n\t"
    "s_waitcnt vmcnt(9)\n\t"
    "s_cbranch_scc1 LP2%=\n\t"
    "s_barrier\n\t"
    "s_waitcnt vmcnt(0)\n\t"
    "s_barrier\n\t"
    "s_mov_b64 s[54:55], s[72:73]\n\t"
    "s_mov_b32 s56, s18\n\t"
    "s_lshl_b32 s57, s36, 13\n\t"
    "s_add_u32 s57, s57, 66048\n\t"
    "v_and_b32 v90, 7, v251\n\t"
    "v_lshlrev_b32 v90, 10, v90\n\t"
    "s_mul_i32 s58, s36, 40960\n\t"
    "s_lshl_b32 s38, s56, 10\n\t"
    "s_add_u32 s58, s58, s38\n\t"
    "s_add_u32 s58, s58, 4259840\n\t"
    "v_lshlrev_b32 v91, 4, v251\n\t"
    "s_add_u32 s38, s34, 1\n\t"
    "s_and_b32 s38, s38, 7\n\t"
    "s_mul_i32 s38, s38, 5120\n\t"
    "s_add_u32 s61, s58, s38\n\t"
    "s_add_u32 s38, s34, 2\n\t"
    "s_and_b32 s38, s38, 7\n\t"
    "s_mul_i32 s38, s38, 5120\n\t"
    "s_add_u32 s62, s58, s38\n\t"
    "s_add_u32 s38, s34, 3\n\t"
    "s_and_b32 s38, s38, 7\n\t"
    "s_mul_i32 s38, s38, 5120\n\t"
    "s_add_u32 s63, s58, s38\n\t"
    "s_add_u32 s38, s34, 4\n\t"
    "s_and_b32 s38, s38, 7\n\t"
    "s_mul_i32 s38, s38, 5120\n\t"
    "s_add_u32 s64, s58, s38\n\t"
    "s_add_u32 s38, s34, 5\n\t"
    "s_and_b32 s38, s38, 7\n\t"
    "s_mul_i32 s38, s38, 5120\n\t"
    "s_add_u32 s65, s58, s38\n\t"
    "s_add_u32 s38, s34, 6\n\t"
    "s_and_b32 s38, s38, 7\n\t"
    "s_mul_i32 s38, s38, 5120\n\t"
    "s_add_u32 s66, s58, s38\n\t"
    "s_add_u32 s38, s34, 7\n\t"
    "s_and_b32 s38, s38, 7\n\t"
    "s_mul_i32 s38, s38, 5120\n\t"
    "s_add_u32 s67, s58, s38\n\t"
    "s_lshl_b32 s38, s56, 10\n\t"
    "s_cmp_eq_u32 s56, 4\n\t"
    "s_cselect_b32 s38, 40960, s38\n\t"
    "s_add_u32 s38, s38, 36864\n\t"
    "v_add_u32 v98, s38, v91\n\t"
    "s_lshl_b32 s38, s29, 3\n\t"
    "s_add_u32 s38, s38, s34\n\t"
    "s_mul_i32 s38, s38, 40960\n\t"
    "s_mul_i32 s39, s19, 1280\n\t"
    "s_add_u32 s38, s38, s39\n\t"
    "s_mul_i32 s39, s35, 320\n\t"
    "s_add_u32 s38, s38, s39\n\t"
    "s_lshl_b32 s39, s56, 6\n\t"
    "s_add_u32 s38, s38, s39\n\t"
    "s_add_u32 s20, s20, s38\n\t"
    "s_addc_u32 s21, s21, 0\n\t"
    "v_and_b32 v92, 15, v251\n\t"
    "v_mul_u32_u24 v92, 0xa00, v92\n\t"
    "v_lshrrev_b32 v93, 4, v251\n\t"
    "v_lshl_add_u32 v92, v93, 3, v92\n\t"
    "v_and_b32 v93, 1, v93\n\t"
    "v_mul_u32_u24 v93, 24, v93\n\t"
    "v_add_u32 v99, v92, v93\n\t"
    "s_mov_b32 s59, 0x40000\n\t"
    "s_barrier\n\t"
    "RED%=:\n\t"
    "s_mov_b32 s80, 0\n\t"
    "ds_read_b128 v[100:103], v98\n\t"
    "REDP%=:\n\t"
    "s_sleep 2\n\t"
    "buffer_load_dwordx2 v[94:95], v90, s[24:27], s57 offen sc1\n\t"
    "s_waitcnt vmcnt(0)\n\t"
    "v_cmp_ne_u64 vcc, s[54:55], v[94:95]\n\t"
    "s_cbranch_vccz REDG%=\n\t"
    "s_sub_u32 s59, s59, 1\n\t"
    "s_cbranch_scc0 REDP%=\n\t"
    "REDG%=:\n\t"
    "buffer_load_dwordx4 v[104:107], v91, s[24:27], s61 offen sc1\n\t"
    "buffer_load_dwordx4 v[108:111], v91, s[24:27], s62 offen sc1\n\t"
    "buffer_load_dwordx4 v[112:115], v91, s[24:27], s63 offen sc1\n\t"
    "buffer_load_dwordx4 v[116:119], v91, s[24:27], s64 offen sc1\n\t"
    "buffer_load_dwordx4 v[120:123], v91, s[24:27], s65 offen sc1\n\t"
    "buffer_load_dwordx4 v[124:127], v91, s[24:27], s66 offen sc1\n\t"
    "buffer_load_dwordx4 v[128:131], v91, s[24:27], s67 offen sc1\n\t"
    "REDT%=:\n\t"
    "s_waitcnt lgkmcnt(0)\n\t"
    "v_lshlrev_b32 v80, 16, v100\n\t"
    "v_and_b32 v81, 0xffff0000, v100\n\t"
    "v_lshlrev_b32 v82, 16, v101\n\t"
    "v_and_b32 v83, 0xffff0000, v101\n\t"
    "v_lshlrev_b32 v84, 16, v102\n\t"
    "v_and_b32 v85, 0xffff0000, v102\n\t"
    "v_lshlrev_b32 v86, 16, v103\n\t"
    "v_and_b32 v87, 0xffff0000, v103\n\t"
    "s_mov_b32 s38, 0xffff0000\n\t"
    "s_waitcnt vmcnt(6)\n\t"
    "v_lshlrev_b32 v96, 16, v104\n\t"
    "v_and_b32 v97, s38, v104\n\t"
    "v_add_f32 v80, v80, v96\n\t"
    "v_add_f32 v81, v81, v97\n\t"
    "v_lshlrev_b32 v96, 16, v105\n\t"
    "v_and_b32 v97, s38, v105\n\t"
    "v_add_f32 v82, v82, v96\n\t"
    "v_add_f32 v83, v83, v97\n\t"
    "v_lshlrev_b32 v96, 16, v106\n\t"
    "v_and_b32 v97, s38, v106\n\t"
    "v_add_f32 v84, v84, v96\n\t"
    "v_add_f32 v85, v85, v97\n\t"
    "v_lshlrev_b32 v96, 16, v107\n\t"
    "v_and_b32 v97, s38, v107\n\t"
    "v_add_f32 v86, v86, v96\n\t"
    "v_add_f32 v87, v87, v97\n\t"
    "s_waitcnt vmcnt(5)\n\t"
    "v_lshlrev_b32 v96, 16, v108\n\t"
    "v_and_b32 v97, s38, v108\n\t"
    "v_add_f32 v80, v80, v96\n\t"
    "v_add_f32 v81, v81, v97\n\t"
    "v_lshlrev_b32 v96, 16, v109\n\t"
    "v_and_b32 v97, s38, v109\n\t"
    "v_add_f32 v82, v82, v96\n\t"
    "v_add_f32 v83, v83, v97\n\t"
    "v_lshlrev_b32 v96, 16, v110\n\t"
    "v_and_b32 v97, s38, v110\n\t"
    "v_add_f32 v84, v84, v96\n\t"
    "v_add_f32 v85, v85, v97\n\t"
    "v_lshlrev_b32 v96, 16, v111\n\t"
    "v_and_b32 v97, s38, v111\n\t"
    "v_add_f32 v86, v86, v96\n\t"
    "v_add_f32 v87, v87, v97\n\t"
    "s_waitcnt vmcnt(4)\n\t"
    "v_lshlrev_b32 v96, 16, v112\n\t"
    "v_and_b32 v97, s38, v112\n\t"
    "v_add_f32 v80, v80, v96\n\t"
    "v_add_f32 v81, v81, v97\n\t"
    "v_lshlrev_b32 v96, 16, v113\n\t"
    "v_and_b32 v97, s38, v113\n\t"
    "v_add_f32 v82, v82, v96\n\t"
    "v_add_f32 v83, v83, v97\n\t"
    "v_lshlrev_b32 v96, 16, v114\n\t"
    "v_and_b32 v97, s38, v114\n\t"
    "v_add_f32 v84, v84, v96\n\t"
    "v_add_f32 v85, v85, v97\n\t"
    "v_lshlrev_b32 v96, 16, v115\n\t"
    "v_and_b32 v97, s38, v115\n\t"
    "v_add_f32 v86, v86, v96\n\t"
    "v_add_f32 v87, v87, v97\n\t"
    "s_waitcnt vmcnt(3)\n\t"
    "v_lshlrev_b32 v96, 16, v116\n\t"
    "v_and_b32 v97, s38, v116\n\t"
    "v_add_f32 v80, v80, v96\n\t"
    "v_add_f32 v81, v81, v97\n\t"
    "v_lshlrev_b32 v96, 16, v117\n\t"
    "v_and_b32 v97, s38, v117\n\t"
    "v_add_f32 v82, v82, v96\n\t"
    "v_add_f32 v83, v83, v97\n\t"
    "v_lshlrev_b32 v96, 16, v118\n\t"
    "v_and_b32 v97, s38, v118\n\t"
    "v_add_f32 v84, v84, v96\n\t"
    "v_add_f32 v85, v85, v97\n\t"
    "v_lshlrev_b32 v96, 16, v119\n\t"
    "v_and_b32 v97, s38, v119\n\t"
    "v_add_f32 v86, v86, v96\n\t"
    "v_add_f32 v87, v87, v97\n\t"
    "s_waitcnt vmcnt(2)\n\t"
    "v_lshlrev_b32 v96, 16, v120\n\t"
    "v_and_b32 v97, s38, v120\n\t"
    "v_add_f32 v80, v80, v96\n\t"
    "v_add_f32 v81, v81, v97\n\t"
    "v_lshlrev_b32 v96, 16, v121\n\t"
    "v_and_b32 v97, s38, v121\n\t"
    "v_add_f32 v82, v82, v96\n\t"
    "v_add_f32 v83, v83, v97\n\t"
    "v_lshlrev_b32 v96, 16, v122\n\t"
    "v_and_b32 v97, s38, v122\n\t"
    "v_add_f32 v84, v84, v96\n\t"
    "v_add_f32 v85, v85, v97\n\t"
    "v_lshlrev_b32 v96, 16, v123\n\t"
    "v_and_b32 v97, s38, v123\n\t"
    "v_add_f32 v86, v86, v96\n\t"
    "v_add_f32 v87, v87, v97\n\t"
    "s_waitcnt vmcnt(1)\n\t"
    "v_lshlrev_b32 v96, 16, v124\n\t"
    "v_and_b32 v97, s38, v124\n\t"
    "v_add_f32 v80, v80, v96\n\t"
    "v_add_f32 v81, v81, v97\n\t"
    "v_lshlrev_b32 v96, 16, v125\n\t"
    "v_and_b32 v97, s38, v125\n\t"
    "v_add_f32 v82, v82, v96\n\t"
    "v_add_f32 v83, v83, v97\n\t"
    "v_lshlrev_b32 v96, 16, v126\n\t"
    "v_and_b32 v97, s38, v126\n\t"
    "v_add_f32 v84, v84, v96\n\t"
    "v_add_f32 v85, v85, v97\n\t"
    "v_lshlrev_b32 v96, 16, v127\n\t"
    "v_and_b32 v97, s38, v127\n\t"
    "v_add_f32 v86, v86, v96\n\t"
    "v_add_f32 v87, v87, v97\n\t"
    "s_waitcnt vmcnt(0)\n\t"
    "v_lshlrev_b32 v96, 16, v128\n\t"
    "v_and_b32 v97, s38, v128\n\t"
    "v_add_f32 v80, v80, v96\n\t"
    "v_add_f32 v81, v81, v97\n\t"
    "v_lshlrev_b32 v96, 16, v129\n\t"
    "v_and_b32 v97, s38, v129\n\t"
    "v_add_f32 v82, v82, v96\n\t"
    "v_add_f32 v83, v83, v97\n\t"
    "v_lshlrev_b32 v96, 16, v130\n\t"
    "v_and_b32 v97, s38, v130\n\t"
    "v_add_f32 v84, v84, v96\n\t"
    "v_add_f32 v85, v85, v97\n\t"
    "v_lshlrev_b32 v96, 16, v131\n\t"
    "v_and_b32 v97, s38, v131\n\t"
    "v_add_f32 v86, v86, v96\n\t"
    "v_add_f32 v87, v87, v97\n\t"
    "v_cvt_pk_bf16_f32 v80, v80, v81\n\t"
    "v_cvt_pk_bf16_f32 v81, v82, v83\n\t"
    "v_cvt_pk_bf16_f32 v82, v84, v85\n\t"
    "v_cvt_pk_bf16_f32 v83, v86, v87\n\t"
    "s_nop 1\n\t"
    "v_permlane16_swap_b32 v80, v82\n\t"
    "v_permlane16_swap_b32 v81, v83\n\t"
    "global_store_dwordx4 v99, v[80:83], s[20:21]\n\t"
    "s_cmp_eq_u32 s37, 0x140\n\t"
    "s_cbranch_scc0 REDE%=\n\t"
    "s_lshl_b32 s38, s36, 7\n\t"
    "s_mov_b64 exec, 1\n\t"
    "buffer_store_dwordx2 v[94:95], v90, s[24:27], s38 offen sc1\n\t"
    "s_mov_b64 exec, -1\n\t"
    "REDE%=:\n\t"
    "s_endpgm\n\t"
    "LK0%=:\n\t"
    "s_add_u32 m0, s70, 0\n\t"
    "s_nop 0\n\t"
    "global_load_lds_dwordx4 v7, s[48:49]\n\t"
    "s_add_u32 m0, s70, 1024\n\t"
    "s_nop 0\n\t"
    "global_load_lds_dwordx4 v8, s[48:49]\n\t"
    "s_add_u32 m0, s70, 2048\n\t"
    "s_nop 0\n\t"
    "global_load_lds_dwordx4 v9, s[48:49]\n\t"
    "s_add_u32 m0, s70, 3072\n\t"
    "s_nop 0\n\t"
    "global_load_lds_dwordx4 v10, s[48:49]\n\t"
    "s_add_u32 m0, s70, 16256\n\t"
    "s_nop 0\n\t"
    "global_load_lds_dwordx4 v7, s[48:49] offset:128\n\t"
    "s_add_u32 m0, s70, 17280\n\t"
    "s_nop 0\n\t"
    "global_load_lds_dwordx4 v8, s[48:49] offset:128\n\t"
    "s_add_u32 m0, s70, 18304\n\t"
    "s_nop 0\n\t"
    "global_load_lds_dwordx4 v9, s[48:49] offset:128\n\t"
    "s_add_u32 m0, s70, 19328\n\t"
    "s_nop 0\n\t"
    "global_load_lds_dwordx4 v10, s[48:49] offset:128\n\t"
    "s_and_b32 s38, s64, 15\n\t"
    "s_lshr_b64 s[64:65], s[64:65], 4\n\t"
    "s_mul_i32 s39, s38, 0xc0000\n\t"
    "s_add_u32 s44, s40, s39\n\t"
    "s_addc_u32 s45, s41, 0\n\t"
    "s_lshl_b32 s38, s38, 7\n\t"
    "s_add_u32 s46, s42, s38\n\t"
    "s_addc_u32 s47, s43, 0\n\t"
    "s_add_u32 s58, s58, 40960\n\t"
    "s_cmp_eq_u32 s58, 122880\n\t"
    "s_cselect_b32 s58, 0, s58\n\t"
    "s_add_u32 s54, s56, s58\n\t"
    "s_add_u32 s55, s57, s58\n\t"
    "s_waitcnt vmcnt(18)\n\t"
    "s_branch LP1B%=\n\t"
    "LK12%=:\n\t"
    "s_add_u32 m0, s70, -256\n\t"
    "s_nop 0\n\t"
    "global_load_lds_dwordx4 v7, s[48:49] offset:256\n\t"
    "s_add_u32 m0, s70, 768\n\t"
    "s_nop 0\n\t"
    "global_load_lds_dwordx4 v8, s[48:49] offset:256\n\t"
    "s_add_u32 m0, s70, 1792\n\t"
    "s_nop 0\n\t"
    "global_load_lds_dwordx4 v9, s[48:49] offset:256\n\t"
    "s_add_u32 m0, s70, 2816\n\t"
    "s_nop 0\n\t"
    "global_load_lds_dwordx4 v10, s[48:49] offset:256\n\t"
    "s_and_b32 s38, s64, 15\n\t"
    "s_lshr_b64 s[64:65], s[64:65], 4\n\t"
    "s_mul_i32 s39, s38, 0xc0000\n\t"
    "s_add_u32 s44, s40, s39\n\t"
    "s_addc_u32 s45, s41, 0\n\t"
    "s_lshl_b32 s38, s38, 7\n\t"
    "s_add_u32 s46, s42, s38\n\t"
    "s_addc_u32 s47, s43, 0\n\t"
    "s_add_u32 s58, s58, 40960\n\t"
    "s_cmp_eq_u32 s58, 122880\n\t"
    "s_cselect_b32 s58, 0, s58\n\t"
    "s_add_u32 s54, s56, s58\n\t"
    "s_add_u32 s55, s57, s58\n\t"
    "s_waitcnt vmcnt(14)\n\t"
    "s_branch LP1B%=\n\t"
    "LE0%=:\n\t"
    "s_mov_b32 s38, 0x9e3779b1\n\t"
    "s_mov_b32 s39, 0x85ebca77\n\t"
    "v_mul_lo_u32 v18, v18, s38\n\t"
    "v_mul_lo_u32 v19, v19, s39\n\t"
    "v_add_u32 v18, 0x632be5ab, v18\n\t"
    "v_add_u32 v19, 0xd9b4e019, v19\n\t"
    "v_lshlrev_b32 v17, 4, v251\n\t"
    "v_and_b32 v29, 3, v251\n\t"
    "v_lshlrev_b32 v29, 5, v29\n\t"
    "v_readfirstlane_b32 s72, v18\n\t"
    "v_readfirstlane_b32 s73, v19\n\t"
    "s_lshl_b32 s38, s36, 4\n\t"
    "s_add_u32 s38, s38, s18\n\t"
    "s_lshl_b32 s38, s38, 3\n\t"
    "s_add_u32 s76, s38, 32768\n\t"
    "s_lshl_b32 s38, s19, 2\n\t"
    "s_add_u32 s38, s38, s29\n\t"
    "s_lshl_b32 s38, s38, 2\n\t"
    "s_add_u32 s38, s38, s18\n\t"
    "s_lshl_b32 s38, s38, 3\n\t"
    "s_add_u32 s38, s38, s34\n\t"
    "s_lshl_b32 s38, s38, 13\n\t"
    "s_add_u32 s38, s38, 2162688\n\t"
    "s_add_u32 s74, s22, s38\n\t"
    "s_addc_u32 s75, s23, 0\n\t"
    "s_mul_i32 s77, s18, 40960\n\t"
    "s_add_u32 s77, s77, 36864\n\t"
    "s_mov_b32 s78, 17\n\t"
    "s_waitcnt vmcnt(9)\n\t"
    "s_branch LP2%=\n\t"
    "LE1%=:\n\t"
    "s_mov_b32 s78, 18\n\t"
    "s_waitcnt vmcnt(9)\n\t"
    "s_barrier\n\t"
    "buffer_load_dwordx2 v[30:31], v29, s[24:27], s76 offen sc1\n\t"
    "s_branch LP2S%=\n\t"
    "LEX%=:\n\t"
    "s_cmp_eq_u32 s59, 11\n\t"
    "s_cbranch_scc1 LE0%=\n\t"
    "s_cmp_eq_u32 s59, 17\n\t"
    "s_cbranch_scc1 LE1%=\n\t"
    "s_cmp_eq_u32 s59, 21\n\t"
    "s_cbranch_scc1 LE5G%=\n\t"
    "s_waitcnt vmcnt(9)\n\t"
    "s_mov_b32 s79, 0x40000\n\t"
    "LE5L%=:\n\t"
    "v_cmp_ne_u64 vcc, s[72:73], v[30:31]\n\t"
    "s_cbranch_vccz LE5G%=\n\t"
    "buffer_load_dwordx2 v[30:31], v29, s[24:27], s76 offen sc1\n\t"
    "s_waitcnt vmcnt(0)\n\t"
    "s_sub_u32 s79, s79, 1\n\t"
    "s_cbranch_scc0 LE5L%=\n\t"
    "LE5G%=:\n\t"
    "s_mov_b32 m0, s77\n\t"
    "s_nop 0\n\t"
    "global_load_lds_dwordx4 v17, s[74:75] offset:0 sc1\n\t"
    "global_load_lds_dwordx4 v17, s[74:75] offset:1024 sc1\n\t"
    "global_load_lds_dwordx4 v17, s[74:75] offset:2048 sc1\n\t"
    "global_load_lds_dwordx4 v17, s[74:75] offset:3072 sc1\n\t"
    "s_add_u32 s74, s74, 0x1000\n\t"
    "s_addc_u32 s75, s75, 0\n\t"
    "s_mov_b32 s78, 21\n\t"
    "s_waitcnt vmcnt(13)\n\t"
    "s_branch LP2%=\n\t"
    "CONS%=:\n\t"
    "v_lshrrev_b32 v1, 4, v251\n\t"
    "v_and_b32 v2, 15, v251\n\t"
    "v_lshrrev_b32 v3, 2, v2\n\t"
    "v_and_b32 v4, 3, v251\n\t"
    "v_lshlrev_b32 v4, 3, v4\n\t"
    "v_and_b32 v5, 1, v1\n\t"
    "v_lshrrev_b32 v6, 1, v2\n\t"
    "v_mul_u32_u24 v7, 0xc00, v1\n\t"
    "v_lshl_add_u32 v7, v3, 7, v7\n\t"
    "v_and_b32 v8, 2, v3\n\t"
    "v_or_b32 v8, v8, v5\n\t"
    "v_lshl_add_u32 v7, v8, 5, v7\n\t"
    "v_add_u32 v245, v7, v4\n\t"
    "s_lshl_b32 s38, s18, 12\n\t"
    "v_lshl_add_u32 v9, v2, 7, s38\n\t"
    "v_xor_b32 v10, v1, v6\n\t"
    "v_lshl_add_u32 v10, v10, 4, v9\n\t"
    "v_add_u32 v247, 24576, v10\n\t"
    "s_waitcnt lgkmcnt(0)\n\t"
    "s_bfe_u32 s19, %[bid], 0x10002\n\t"
    "s_lshr_b32 s34, s30, 2\n\t"
    "s_and_b32 s35, s30, 3\n\t"
    "s_lshl_b32 s36, s19, 5\n\t"
    "s_add_u32 s36, s36, s30\n\t"
    "s_lshl_b32 s36, s36, 2\n\t"
    "s_add_u32 s36, s36, s29\n\t"
    "s_mov_b32 s24, s22\n\t"
    "s_and_b32 s25, s23, 0xffff\n\t"
    "s_mov_b32 s26, 0xe10000\n\t"
    "s_mov_b32 s27, 0x20000\n\t"
    "s_mov_b32 s60, 0xc92db1\n\t"
    "s_mov_b32 s61, 0x5\n\t"
    "s_cmp_eq_u32 s31, 0\n\t"
    "s_cselect_b32 s60, 0x1b1879, s60\n\t"
    "s_cselect_b32 s61, 0x2, s61\n\t"
    "s_cmp_eq_u32 s31, 1\n\t"
    "s_cselect_b32 s60, 0x4b4d86, s60\n\t"
    "s_cselect_b32 s61, 0x4, s61\n\t"
    "s_cmp_eq_u32 s31, 2\n\t"
    "s_cselect_b32 s60, 0xc9e4d2, s60\n\t"
    "s_cselect_b32 s61, 0x3, s61\n\t"
    "v_lshlrev_b32 v244, 4, v251\n\t"
    "s_and_b32 s38, s36, 0xf3\n\t"
    "v_lshl_add_u32 v13, v251, 2, s38\n\t"
    "s_and_b32 s38, s36, 0x8f\n\t"
    "s_lshl_b32 s39, s18, 5\n\t"
    "s_add_u32 s38, s38, s39\n\t"
    "s_sub_u32 s38, s38, 64\n\t"
    "v_add_u32 v14, s38, v244\n\t"
    "v_mov_b32 v15, s36\n\t"
    "v_cmp_gt_u32 vcc, 6, v251\n\t"
    "s_nop 1\n\t"
    "v_cndmask_b32 v15, v15, v14, vcc\n\t"
    "v_cmp_gt_u32 vcc, 4, v251\n\t"
    "s_nop 1\n\t"
    "v_cndmask_b32 v15, v15, v13, vcc\n\t"
    "v_lshlrev_b32 v254, 7, v15\n\t"
    "buffer_load_dwordx2 v[248:249], v254, s[24:27], 0 offen sc1\n\t"
    "s_mov_b32 s57, 0x444\n\t"
    "s_mov_b32 s42, 40960\n\t"
    "v_xor_b32 v24, 0, v245\n\t"
    "v_xor_b32 v25, 32, v245\n\t"
    "v_xor_b32 v26, 64, v245\n\t"
    "v_xor_b32 v27, 96, v245\n\t"
    "v_mov_b32 v28, v247\n\t"
    "v_xor_b32 v29, 64, v247\n\t"
    "s_barrier\n\t"
    "ds_read_b128 v[128:131], v28 offset:0\n\t"
    "ds_read_b128 v[132:135], v28 offset:2048\n\t"
    "ds_read_b64_tr_b16 v[80:81], v24 offset:0\n\t"
    "ds_read_b64_tr_b16 v[82:83], v24 offset:512\n\t"
    "ds_read_b64_tr_b16 v[84:85], v25 offset:0\n\t"
    "ds_read_b64_tr_b16 v[86:87], v25 offset:512\n\t"
    "ds_read_b64_tr_b16 v[88:89], v26 offset:0\n\t"
    "ds_read_b64_tr_b16 v[90:91], v26 offset:512\n\t"
    "ds_read_b64_tr_b16 v[92:93], v27 offset:0\n\t"
    "ds_read_b64_tr_b16 v[94:95], v27 offset:512\n\t"
    "ds_read_b64_tr_b16 v[96:97], v24 offset:1024\n\t"
    "ds_read_b64_tr_b16 v[98:99], v24 offset:1536\n\t"
    "ds_read_b64_tr_b16 v[100:101], v25 offset:1024\n\t"
    "ds_read_b64_tr_b16 v[102:103], v25 offset:1536\n\t"
    "ds_read_b64_tr_b16 v[104:105], v26 offset:1024\n\t"
    "ds_read_b64_tr_b16 v[106:107], v26 offset:1536\n\t"
    "ds_read_b64_tr_b16 v[108:109], v27 offset:1024\n\t"
    "ds_read_b64_tr_b16 v[110:111], v27 offset:1536\n\t"
    "s_waitcnt lgkmcnt(7)\n\t"
    "v_mfma_f32_16x16x32_bf16 v[144:147], v[80:83], v[128:131], 0\n\t"
    "ds_read_b64_tr_b16 v[112:113], v24 offset:2048\n\t"
    "ds_read_b64_tr_b16 v[114:115], v24 offset:2560\n\t"
    "v_mfma_f32_16x16x32_bf16 v[148:151], v[80:83], v[132:135], 0\n\t"
    "ds_read_b64_tr_b16 v[116:117], v25 offset:2048\n\t"
    "v_add_u32 v28, s42, v28\n\t"
    "v_mfma_f32_16x16x32_bf16 v[156:159], v[84:87], v[132:135], 0\n\t"
    "ds_read_b64_tr_b16 v[118:119], v25 offset:2560\n\t"
    "v_mfma_f32_16x16x32_bf16 v[152:155], v[84:87], v[128:131], 0\n\t"
    "ds_read_b64_tr_b16 v[120:121], v26 offset:2048\n\t"
    "v_mfma_f32_16x16x32_bf16 v[160:163], v[88:91], v[128:131], 0\n\t"
    "ds_read_b64_tr_b16 v[122:123], v26 offset:2560\n\t"
    "v_mfma_f32_16x16x32_bf16 v[164:167], v[88:91], v[132:135], 0\n\t"
    "ds_read_b64_tr_b16 v[124:125], v27 offset:2048\n\t"
    "v_mfma_f32_16x16x32_bf16 v[172:175], v[92:95], v[132:135], 0\n\t"
    "ds_read_b64_tr_b16 v[126:127], v27 offset:2560\n\t"
    "v_mfma_f32_16x16x32_bf16 v[168:171], v[92:95], v[128:131], 0\n\t"
    "ds_read_b128 v[136:139], v29 offset:0\n\t"
    "s_waitcnt lgkmcnt(7)\n\t"
    "v_mfma_f32_16x16x32_bf16 v[176:179], v[96:99], v[128:131], 0\n\t"
    "ds_read_b128 v[140:143], v29 offset:2048\n\t"
    "v_add_u32 v29, s42, v29\n\t"
    "v_mfma_f32_16x16x32_bf16 v[180:183], v[96:99], v[132:135], 0\n\t"
    "ds_read_b64_tr_b16 v[80:81], v24 offset:12288\n\t"
    "v_mfma_f32_16x16x32_bf16 v[188:191], v[100:103], v[132:135], 0\n\t"
    "ds_read_b64_tr_b16 v[82:83], v24 offset:12800\n\t"
    "v_mfma_f32_16x16x32_bf16 v[184:187], v[100:103], v[128:131], 0\n\t"
    "ds_read_b64_tr_b16 v[84:85], v25 offset:12288\n\t"
    "v_mfma_f32_16x16x32_bf16 v[192:195], v[104:107], v[128:131], 0\n\t"
    "ds_read_b64_tr_b16 v[86:87], v25 offset:12800\n\t"
    "v_mfma_f32_16x16x32_bf16 v[196:199], v[104:107], v[132:135], 0\n\t"
    "ds_read_b64_tr_b16 v[88:89], v26 offset:12288\n\t"
    "v_mfma_f32_16x16x32_bf16 v[204:207], v[108:111], v[132:135], 0\n\t"
    "ds_read_b64_tr_b16 v[90:91], v26 offset:12800\n\t"
    "v_mfma_f32_16x16x32_bf16 v[200:203], v[108:111], v[128:131], 0\n\t"
    "ds_read_b64_tr_b16 v[92:93], v27 offset:12288\n\t"
    "v_mfma_f32_16x16x32_bf16 v[208:211], v[112:115], v[128:131], 0\n\t"
    "ds_read_b64_tr_b16 v[94:95], v27 offset:12800\n\t"
    "v_mfma_f32_16x16x32_bf16 v[212:215], v[112:115], v[132:135], 0\n\t"
    "ds_read_b64_tr_b16 v[96:97], v24 offset:13312\n\t"
    "s_waitcnt lgkmcnt(7)\n\t"
    "v_mfma_f32_16x16x32_bf16 v[220:223], v[116:119], v[132:135], 0\n\t"
    "ds_read_b64_tr_b16 v[98:99], v24 offset:13824\n\t"
    "v_mfma_f32_16x16x32_bf16 v[216:219], v[116:119], v[128:131], 0\n\t"
    "ds_read_b64_tr_b16 v[100:101], v25 offset:13312\n\t"
    "v_mfma_f32_16x16x32_bf16 v[224:227], v[120:123], v[128:131], 0\n\t"
    "ds_read_b64_tr_b16 v[102:103], v25 offset:13824\n\t"
    "v_mfma_f32_16x16x32_bf16 v[228:231], v[120:123], v[132:135], 0\n\t"
    "ds_read_b64_tr_b16 v[104:105], v26 offset:13312\n\t"
    "v_mfma_f32_16x16x32_bf16 v[236:239], v[124:127], v[132:135], 0\n\t"
    "ds_read_b64_tr_b16 v[106:107], v26 offset:13824\n\t"
    "v_mfma_f32_16x16x32_bf16 v[232:235], v[124:127], v[128:131], 0\n\t"
    "ds_read_b64_tr_b16 v[108:109], v27 offset:13312\n\t"
    "v_mfma_f32_16x16x32_bf16 v[144:147], v[80:83], v[136:139], v[144:147]\n\t"
    "ds_read_b64_tr_b16 v[110:111], v27 offset:13824\n\t"
    "v_mfma_f32_16x16x32_bf16 v[148:151], v[80:83], v[140:143], v[148:151]\n\t"
    "ds_read_b64_tr_b16 v[112:113], v24 offset:14336\n\t"
    "s_waitcnt lgkmcnt(7)\n\t"
    "v_mfma_f32_16x16x32_bf16 v[156:159], v[84:87], v[140:143], v[156:159]\n\t"
    "ds_read_b64_tr_b16 v[114:115], v24 offset:14848\n\t"
    "v_add_u32 v24, s42, v24\n\t"
    "v_mfma_f32_16x16x32_bf16 v[152:155], v[84:87], v[136:139], v[152:155]\n\t"
    "ds_read_b64_tr_b16 v[116:117], v25 offset:14336\n\t"
    "v_mfma_f32_16x16x32_bf16 v[160:163], v[88:91], v[136:139], v[160:163]\n\t"
    "ds_read_b64_tr_b16 v[118:119], v25 offset:14848\n\t"
    "v_add_u32 v25, s42, v25\n\t"
    "v_mfma_f32_16x16x32_bf16 v[164:167], v[88:91], v[140:143], v[164:167]\n\t"
    "ds_read_b64_tr_b16 v[120:121], v26 offset:14336\n\t"
    "v_mfma_f32_16x16x32_bf16 v[172:175], v[92:95], v[140:143], v[172:175]\n\t"
    "ds_read_b64_tr_b16 v[122:123], v26 offset:14848\n\t"
    "v_add_u32 v26, s42, v26\n\t"
    "v_mfma_f32_16x16x32_bf16 v[168:171], v[92:95], v[136:139], v[168:171]\n\t"
    "ds_read_b64_tr_b16 v[124:125], v27 offset:14336\n\t"
    "v_mfma_f32_16x16x32_bf16 v[176:179], v[96:99], v[136:139], v[176:179]\n\t"
    "ds_read_b64_tr_b16 v[126:127], v27 offset:14848\n\t"
    "v_add_u32 v27, s42, v27\n\t"
    "v_mfma_f32_16x16x32_bf16 v[180:183], v[96:99], v[140:143], v[180:183]\n\t"
    "s_waitcnt lgkmcnt(7)\n\t"
    "v_mfma_f32_16x16x32_bf16 v[188:191], v[100:103], v[140:143], v[188:191]\n\t"
    "s_lshl_b32 s38, s19, 2\n\t"
    "s_add_u32 s38, s38, s29\n\t"
    "s_lshl_b32 s38, s38, 2\n\t"
    "s_add_u32 s38, s38, s18\n\t"
    "s_lshl_b32 s38, s38, 3\n\t"
    "s_add_u32 s38, s38, s34\n\t"
    "s_lshl_b32 s38, s38, 13\n\t"
    "s_add_u32 s63, s38, 2162688\n\t"
    "s_lshl_b32 s38, s35, 11\n\t"
    "s_add_u32 s62, s63, s38\n\t"
    "s_lshl_b32 s38, s35, 2\n\t"
    "s_add_u32 s38, s38, s18\n\t"
    "s_lshl_b32 s38, s38, 3\n\t"
    "s_add_u32 s65, s38, 32768\n\t"
    "s_and_b32 s38, s36, 0x8f\n\t"
    "s_lshl_b32 s39, s18, 5\n\t"
    "s_add_u32 s38, s38, s39\n\t"
    "s_lshl_b32 s38, s38, 3\n\t"
    "s_add_u32 s38, s38, s34\n\t"
    "s_mul_i32 s38, s38, 5120\n\t"
    "s_add_u32 s66, s38, 4259840\n\t"
    "s_lshl_b32 s38, s34, 10\n\t"
    "s_add_u32 s67, s38, 65536\n\t"
    "s_add_u32 s70, s66, 655360\n\t"
    "s_lshl_b32 s38, s18, 1\n\t"
    "s_sub_u32 s71, s34, s38\n\t"
    "s_mov_b64 s[72:73], s[24:25]\n\t"
    "s_mov_b32 s75, s27\n\t"
    "s_mov_b64 s[76:77], s[24:25]\n\t"
    "s_mov_b32 s79, s27\n\t"
    "s_cmp_eq_u32 s71, 0\n\t"
    "s_cselect_b32 s74, 0, s26\n\t"
    "s_cmp_eq_u32 s71, 1\n\t"
    "s_cselect_b32 s78, 0, s26\n\t"
    "v_lshrrev_b32 v11, 1, v1\n\t"
    "v_xor_b32 v11, v11, v6\n\t"
    "v_lshl_add_u32 v11, v11, 4, v9\n\t"
    "v_lshl_add_u32 v11, v5, 3, v11\n\t"
    "v_add_u32 v246, 122880, v11\n\t"
    "s_and_b32 s38, s60, 3\n\t"
    "s_mul_i32 s38, s38, 40960\n\t"
    "v_add_u32 v242, s38, v10\n\t"
    "v_xor_b32 v243, 64, v242\n\t"
    "v_lshl_add_u32 v12, v1, 3, v3\n\t"
    "v_mul_u32_u24 v12, 0x140, v12\n\t"
    "v_lshl_add_u32 v12, v5, 5, v12\n\t"
    "v_add_u32 v12, v12, v4\n\t"
    "s_add_u32 s38, s38, 16384\n\t"
    "v_add_u32 v240, s38, v12\n\t"
    "v_xor_b32 v241, 32, v240\n\t"
    "s_mul_i32 s38, s18, 40960\n\t"
    "s_add_u32 s38, s38, 36864\n\t"
    "v_add_u32 v245, s38, v244\n\t"
    "s_mov_b32 s40, 1\n\t"
    "s_mov_b32 s41, 40960\n\t"
    "s_mov_b32 s43, 40960\n\t"
    "s_mov_b32 s48, 0x3dd53b95\n\t"
    "s_mov_b32 s49, 0xbdd53b95\n\t"
    "v_xor_b32 v20, 0, v246\n\t"
    "v_xor_b32 v21, 32, v246\n\t"
    "v_xor_b32 v22, 64, v246\n\t"
    "v_xor_b32 v23, 96, v246\n\t"
    "s_waitcnt lgkmcnt(0)\n\t"
    "P1%=:\n\t"
    "v_mfma_f32_16x16x32_bf16 v[184:187], v[100:103], v[136:139], v[184:187]\n\t"
    "s_cmp_eq_u32 s40, s61\n\t"
    "s_waitcnt lgkmcnt(0)\n\t"
    "s_barrier\n\t"
    "v_mfma_f32_16x16x32_bf16 v[192:195], v[104:107], v[136:139], v[192:195]\n\t"
    "s_cbranch_scc1 PICK%=\n\t"
    "PICKB%=:\n\t"
    "ds_read_b128 v[128:131], v28 offset:0\n\t"
    "ds_read_b128 v[132:135], v28 offset:2048\n\t"
    "v_mfma_f32_16x16x32_bf16 v[196:199], v[104:107], v[140:143], v[196:199]\n\t"
    "ds_read_b64_tr_b16 v[80:81], v24 offset:0\n\t"
    "ds_read_b64_tr_b16 v[82:83], v24 offset:512\n\t"
    "v_mfma_f32_16x16x32_bf16 v[204:207], v[108:111], v[140:143], v[204:207]\n\t"
    "ds_read_b64_tr_b16 v[84:85], v25 offset:0\n\t"
    "ds_read_b64_tr_b16 v[86:87], v25 offset:512\n\t"
    "v_mfma_f32_16x16x32_bf16 v[200:203], v[108:111], v[136:139], v[200:203]\n\t"
    "ds_read_b64_tr_b16 v[88:89], v26 offset:0\n\t"
    "ds_read_b64_tr_b16 v[90:91], v26 offset:512\n\t"
    "v_mfma_f32_16x16x32_bf16 v[208:211], v[112:115], v[136:139], v[208:211]\n\t"
    "ds_read_b64_tr_b16 v[92:93], v27 offset:0\n\t"
    "ds_read_b64_tr_b16 v[94:95], v27 offset:512\n\t"
    "v_mfma_f32_16x16x32_bf16 v[212:215], v[112:115], v[140:143], v[212:215]\n\t"
    "ds_read_b64_tr_b16 v[96:97], v24 offset:1024\n\t"
    "ds_read_b64_tr_b16 v[98:99], v24 offset:1536\n\t"
    "v_mfma_f32_16x16x32_bf16 v[220:223], v[116:119], v[140:143], v[220:223]\n\t"
    "ds_read_b64_tr_b16 v[100:101], v25 offset:1024\n\t"
    "ds_read_b64_tr_b16 v[102:103], v25 offset:1536\n\t"
    "v_mfma_f32_16x16x32_bf16 v[216:219], v[116:119], v[136:139], v[216:219]\n\t"
    "ds_read_b64_tr_b16 v[104:105], v26 offset:1024\n\t"
    "s_cmp_eq_u32 s41, 81920\n\t"
    "v_mfma_f32_16x16x32_bf16 v[224:227], v[120:123], v[136:139], v[224:227]\n\t"
    "ds_read_b64_tr_b16 v[106:107], v26 offset:1536\n\t"
    "s_cselect_b32 s42, 0xfffec000, s43\n\t"
    "v_mfma_f32_16x16x32_bf16 v[228:231], v[120:123], v[140:143], v[228:231]\n\t"
    "ds_read_b64_tr_b16 v[108:109], v27 offset:1024\n\t"
    "s_add_u32 s41, s41, s42\n\t"
    "v_mfma_f32_16x16x32_bf16 v[236:239], v[124:127], v[140:143], v[236:239]\n\t"
    "ds_read_b64_tr_b16 v[110:111], v27 offset:1536\n\t"
    "v_add_u32 v28, s42, v28\n\t"
    "v_mfma_f32_16x16x32_bf16 v[232:235], v[124:127], v[136:139], v[232:235]\n\t"
    "ds_read_b64_tr_b16 v[112:113], v24 offset:2048\n\t"
    "s_waitcnt lgkmcnt(7)\n\t"
    "v_mfma_f32_16x16x32_bf16 v[144:147], v[80:83], v[128:131], v[144:147]\n\t"
    "ds_read_b64_tr_b16 v[114:115], v24 offset:2560\n\t"
    "v_mfma_f32_16x16x32_bf16 v[148:151], v[80:83], v[132:135], v[148:151]\n\t"
    "ds_read_b64_tr_b16 v[116:117], v25 offset:2048\n\t"
    "s_cmp_lg_u32 s57, 0\n\t"
    "s_cbranch_scc1 PACE%=\n\t"
    "PACEB%=:\n\t"
    "v_mfma_f32_16x16x32_bf16 v[156:159], v[84:87], v[132:135], v[156:159]\n\t"
    "ds_read_b64_tr_b16 v[118:119], v25 offset:2560\n\t"
    "s_add_u32 s40, s40, 1\n\t"
    "v_mfma_f32_16x16x32_bf16 v[152:155], v[84:87], v[128:131], v[152:155]\n\t"
    "ds_read_b64_tr_b16 v[120:121], v26 offset:2048\n\t"
    "s_cmp_eq_u32 s40, 10\n\t"
    "v_mfma_f32_16x16x32_bf16 v[160:163], v[88:91], v[128:131], v[160:163]\n\t"
    "ds_read_b64_tr_b16 v[122:123], v26 offset:2560\n\t"
    "v_mfma_f32_16x16x32_bf16 v[164:167], v[88:91], v[132:135], v[164:167]\n\t"
    "ds_read_b64_tr_b16 v[124:125], v27 offset:2048\n\t"
    "v_mfma_f32_16x16x32_bf16 v[172:175], v[92:95], v[132:135], v[172:175]\n\t"
    "ds_read_b64_tr_b16 v[126:127], v27 offset:2560\n\t"
    "v_mfma_f32_16x16x32_bf16 v[168:171], v[92:95], v[128:131], v[168:171]\n\t"
    "ds_read_b128 v[136:139], v29 offset:0\n\t"
    "v_mfma_f32_16x16x32_bf16 v[176:179], v[96:99], v[128:131], v[176:179]\n\t"
    "ds_read_b128 v[140:143], v29 offset:2048\n\t"
    "v_add_u32 v29, s42, v29\n\t"
    "v_mfma_f32_16x16x32_bf16 v[180:183], v[96:99], v[132:135], v[180:183]\n\t"
    "ds_read_b64_tr_b16 v[80:81], v24 offset:12288\n\t"
    "s_waitcnt lgkmcnt(7)\n\t"
    "v_mfma_f32_16x16x32_bf16 v[188:191], v[100:103], v[132:135], v[188:191]\n\t"
    "ds_read_b64_tr_b16 v[82:83], v24 offset:12800\n\t"
    "v_mfma_f32_16x16x32_bf16 v[184:187], v[100:103], v[128:131], v[184:187]\n\t"
    "ds_read_b64_tr_b16 v[84:85], v25 offset:12288\n\t"
    "v_mfma_f32_16x16x32_bf16 v[192:195], v[104:107], v[128:131], v[192:195]\n\t"
    "ds_read_b64_tr_b16 v[86:87], v25 offset:12800\n\t"
    "v_mfma_f32_16x16x32_bf16 v[196:199], v[104:107], v[132:135], v[196:199]\n\t"
    "ds_read_b64_tr_b16 v[88:89], v26 offset:12288\n\t"
    "v_mfma_f32_16x16x32_bf16 v[204:207], v[108:111], v[132:135], v[204:207]\n\t"
    "ds_read_b64_tr_b16 v[90:91], v26 offset:12800\n\t"
    "v_mfma_f32_16x16x32_bf16 v[200:203], v[108:111], v[128:131], v[200:203]\n\t"
    "ds_read_b64_tr_b16 v[92:93], v27 offset:12288\n\t"
    "v_mfma_f32_16x16x32_bf16 v[208:211], v[112:115], v[128:131], v[208:211]\n\t"
    "ds_read_b64_tr_b16 v[94:95], v27 offset:12800\n\t"
    "v_mfma_f32_16x16x32_bf16 v[212:215], v[112:115], v[132:135], v[212:215]\n\t"
    "ds_read_b64_tr_b16 v[96:97], v24 offset:13312\n\t"
    "v_mfma_f32_16x16x32_bf16 v[220:223], v[116:119], v[132:135], v[220:223]\n\t"
    "ds_read_b64_tr_b16 v[98:99], v24 offset:13824\n\t"
    "v_mfma_f32_16x16x32_bf16 v[216:219], v[116:119], v[128:131], v[216:219]\n\t"
    "ds_read_b64_tr_b16 v[100:101], v25 offset:13312\n\t"
    "s_waitcnt lgkmcnt(7)\n\t"
    "v_mfma_f32_16x16x32_bf16 v[224:227], v[120:123], v[128:131], v[224:227]\n\t"
    "ds_read_b64_tr_b16 v[102:103], v25 offset:13824\n\t"
    "v_mfma_f32_16x16x32_bf16 v[228:231], v[120:123], v[132:135], v[228:231]\n\t"
    "ds_read_b64_tr_b16 v[104:105], v26 offset:13312\n\t"
    "v_mfma_f32_16x16x32_bf16 v[236:239], v[124:127], v[132:135], v[236:239]\n\t"
    "ds_read_b64_tr_b16 v[106:107], v26 offset:13824\n\t"
    "v_mfma_f32_16x16x32_bf16 v[232:235], v[124:127], v[128:131], v[232:235]\n\t"
    "ds_read_b64_tr_b16 v[108:109], v27 offset:13312\n\t"
    "v_mfma_f32_16x16x32_bf16 v[144:147], v[80:83], v[136:139], v[144:147]\n\t"
    "ds_read_b64_tr_b16 v[110:111], v27 offset:13824\n\t"
    "v_mfma_f32_16x16x32_bf16 v[148:151], v[80:83], v[140:143], v[148:151]\n\t"
    "ds_read_b64_tr_b16 v[112:113], v24 offset:14336\n\t"
    "v_mfma_f32_16x16x32_bf16 v[156:159], v[84:87], v[140:143], v[156:159]\n\t"
    "ds_read_b64_tr_b16 v[114:115], v24 offset:14848\n\t"
    "v_add_u32 v24, s42, v24\n\t"
    "v_mfma_f32_16x16x32_bf16 v[152:155], v[84:87], v[136:139], v[152:155]\n\t"
    "ds_read_b64_tr_b16 v[116:117], v25 offset:14336\n\t"
    "s_waitcnt lgkmcnt(7)\n\t"
    "v_mfma_f32_16x16x32_bf16 v[160:163], v[88:91], v[136:139], v[160:163]\n\t"
    "ds_read_b64_tr_b16 v[118:119], v25 offset:14848\n\t"
    "v_add_u32 v25, s42, v25\n\t"
    "v_mfma_f32_16x16x32_bf16 v[164:167], v[88:91], v[140:143], v[164:167]\n\t"
    "ds_read_b64_tr_b16 v[120:121], v26 offset:14336\n\t"
    "v_mfma_f32_16x16x32_bf16 v[172:175], v[92:95], v[140:143], v[172:175]\n\t"
    "ds_read_b64_tr_b16 v[122:123], v26 offset:14848\n\t"
    "v_add_u32 v26, s42, v26\n\t"
    "v_mfma_f32_16x16x32_bf16 v[168:171], v[92:95], v[136:139], v[168:171]\n\t"
    "ds_read_b64_tr_b16 v[124:125], v27 offset:14336\n\t"
    "v_mfma_f32_16x16x32_bf16 v[176:179], v[96:99], v[136:139], v[176:179]\n\t"
    "ds_read_b64_tr_b16 v[126:127], v27 offset:14848\n\t"
    "v_add_u32 v27, s42, v27\n\t"
    "v_mfma_f32_16x16x32_bf16 v[180:183], v[96:99], v[140:143], v[180:183]\n\t"
    "v_mfma_f32_16x16x32_bf16 v[188:191], v[100:103], v[140:143], v[188:191]\n\t"
    "s_cbranch_scc0 P1%=\n\t"
    "v_mfma_f32_16x16x32_bf16 v[184:187], v[100:103], v[136:139], v[184:187]\n\t"
    "s_waitcnt lgkmcnt(0)\n\t"
    "s_barrier\n\t"
    "v_mfma_f32_16x16x32_bf16 v[192:195], v[104:107], v[136:139], v[192:195]\n\t"
    "ds_read_b64 v[32:33], v20 offset:16384\n\t"
    "v_cvt_pk_bf16_f32 v144, v144, v145\n\t"
    "v_mfma_f32_16x16x32_bf16 v[196:199], v[104:107], v[140:143], v[196:199]\n\t"
    "ds_read_b64 v[34:35], v21 offset:16384\n\t"
    "v_cvt_pk_bf16_f32 v145, v146, v147\n\t"
    "v_mfma_f32_16x16x32_bf16 v[204:207], v[108:111], v[140:143], v[204:207]\n\t"
    "ds_read_b64 v[36:37], v20 offset:18432\n\t"
    "v_cvt_pk_bf16_f32 v146, v152, v153\n\t"
    "v_mfma_f32_16x16x32_bf16 v[200:203], v[108:111], v[136:139], v[200:203]\n\t"
    "ds_read_b64 v[38:39], v21 offset:18432\n\t"
    "v_cvt_pk_bf16_f32 v147, v154, v155\n\t"
    "v_mfma_f32_16x16x32_bf16 v[208:211], v[112:115], v[136:139], v[208:211]\n\t"
    "ds_read_b64 v[40:41], v22 offset:16384\n\t"
    "v_cvt_pk_bf16_f32 v148, v148, v149\n\t"
    "v_mfma_f32_16x16x32_bf16 v[212:215], v[112:115], v[140:143], v[212:215]\n\t"
    "ds_read_b64 v[42:43], v23 offset:16384\n\t"
    "v_cvt_pk_bf16_f32 v149, v150, v151\n\t"
    "v_mfma_f32_16x16x32_bf16 v[220:223], v[116:119], v[140:143], v[220:223]\n\t"
    "ds_read_b64 v[44:45], v22 offset:18432\n\t"
    "v_cvt_pk_bf16_f32 v150, v156, v157\n\t"
    "v_mfma_f32_16x16x32_bf16 v[216:219], v[116:119], v[136:139], v[216:219]\n\t"
    "ds_read_b64 v[46:47], v23 offset:18432\n\t"
    "v_cvt_pk_bf16_f32 v151, v158, v159\n\t"
    "v_mfma_f32_16x16x32_bf16 v[224:227], v[120:123], v[136:139], v[224:227]\n\t"
    "ds_read_b64 v[48:49], v20 offset:0\n\t"
    "v_cvt_pk_bf16_f32 v160, v160, v161\n\t"
    "v_mfma_f32_16x16x32_bf16 v[228:231], v[120:123], v[140:143], v[228:231]\n\t"
    "ds_read_b64 v[50:51], v21 offset:0\n\t"
    "v_cvt_pk_bf16_f32 v161, v162, v163\n\t"
    "v_mfma_f32_16x16x32_bf16 v[236:239], v[124:127], v[140:143], v[236:239]\n\t"
    "ds_read_b64 v[52:53], v20 offset:2048\n\t"
    "v_cvt_pk_bf16_f32 v162, v168, v169\n\t"
    "v_mfma_f32_16x16x32_bf16 v[232:235], v[124:127], v[136:139], v[232:235]\n\t"
    "ds_read_b64 v[54:55], v21 offset:2048\n\t"
    "v_cvt_pk_bf16_f32 v163, v170, v171\n\t"
    "v_mfma_f32_16x16x32_bf16 v[0:3], v[64:67], v[144:147], 0\n\t"
    "ds_read_b64 v[56:57], v22 offset:0\n\t"
    "v_cvt_pk_bf16_f32 v164, v164, v165\n\t"
    "v_mfma_f32_16x16x32_bf16 v[4:7], v[68:71], v[144:147], 0\n\t"
    "ds_read_b64 v[58:59], v23 offset:0\n\t"
    "v_cvt_pk_bf16_f32 v165, v166, v167\n\t"
    "v_mfma_f32_16x16x32_bf16 v[12:15], v[68:71], v[148:151], 0\n\t"
    "ds_read_b64 v[60:61], v22 offset:2048\n\t"
    "v_cvt_pk_bf16_f32 v166, v172, v173\n\t"
    "v_mfma_f32_16x16x32_bf16 v[8:11], v[64:67], v[148:151], 0\n\t"
    "ds_read_b64 v[62:63], v23 offset:2048\n\t"
    "v_cvt_pk_bf16_f32 v167, v174, v175\n\t"
    "v_mfma_f32_16x16x32_bf16 v[0:3], v[72:75], v[160:163], v[0:3]\n\t"
    "v_cvt_pk_bf16_f32 v176, v176, v177\n\t"
    "v_cvt_pk_bf16_f32 v177, v178, v179\n\t"
    "v_mfma_f32_16x16x32_bf16 v[4:7], v[76:79], v[160:163], v[4:7]\n\t"
    "v_cvt_pk_bf16_f32 v178, v184, v185\n\t"
    "v_cvt_pk_bf16_f32 v179, v186, v187\n\t"
    "v_mfma_f32_16x16x32_bf16 v[12:15], v[76:79], v[164:167], v[12:15]\n\t"
    "v_cvt_pk_bf16_f32 v180, v180, v181\n\t"
    "v_cvt_pk_bf16_f32 v181, v182, v183\n\t"
    "v_mfma_f32_16x16x32_bf16 v[8:11], v[72:75], v[164:167], v[8:11]\n\t"
    "v_cvt_pk_bf16_f32 v182, v188, v189\n\t"
    "v_cvt_pk_bf16_f32 v183, v190, v191\n\t"
    "s_waitcnt lgkmcnt(7)\n\t"
    "v_mfma_f32_16x16x32_bf16 v[0:3], v[32:35], v[176:179], v[0:3]\n\t"
    "v_cvt_pk_bf16_f32 v192, v192, v193\n\t"
    "v_cvt_pk_bf16_f32 v193, v194, v195\n\t"
    "v_mfma_f32_16x16x32_bf16 v[4:7], v[36:39], v[176:179], v[4:7]\n\t"
    "v_cvt_pk_bf16_f32 v194, v200, v201\n\t"
    "v_cvt_pk_bf16_f32 v195, v202, v203\n\t"
    "v_mfma_f32_16x16x32_bf16 v[12:15], v[36:39], v[180:183], v[12:15]\n\t"
    "v_cvt_pk_bf16_f32 v196, v196, v197\n\t"
    "v_cvt_pk_bf16_f32 v197, v198, v199\n\t"
    "v_mfma_f32_16x16x32_bf16 v[8:11], v[32:35], v[180:183], v[8:11]\n\t"
    "v_cvt_pk_bf16_f32 v198, v204, v205\n\t"
    "v_cvt_pk_bf16_f32 v199, v206, v207\n\t"
    "v_mfma_f32_16x16x32_bf16 v[0:3], v[40:43], v[192:195], v[0:3]\n\t"
    "v_cvt_pk_bf16_f32 v208, v208, v209\n\t"
    "v_cvt_pk_bf16_f32 v209, v210, v211\n\t"
    "v_mfma_f32_16x16x32_bf16 v[4:7], v[44:47], v[192:195], v[4:7]\n\t"
    "v_cvt_pk_bf16_f32 v210, v216, v217\n\t"
    "v_cvt_pk_bf16_f32 v211, v218, v219\n\t"
    "v_mfma_f32_16x16x32_bf16 v[12:15], v[44:47], v[196:199], v[12:15]\n\t"
    "v_cvt_pk_bf16_f32 v212, v212, v213\n\t"
    "v_cvt_pk_bf16_f32 v213, v214, v215\n\t"
    "v_mfma_f32_16x16x32_bf16 v[8:11], v[40:43], v[196:199], v[8:11]\n\t"
    "v_cvt_pk_bf16_f32 v214, v220, v221\n\t"
    "v_cvt_pk_bf16_f32 v215, v222, v223\n\t"
    "s_waitcnt lgkmcnt(6)\n\t"
    "v_mfma_f32_16x16x32_bf16 v[0:3], v[48:51], v[208:211], v[0:3]\n\t"
    "v_cvt_pk_bf16_f32 v224, v224, v225\n\t"
    "v_cvt_pk_bf16_f32 v225, v226, v227\n\t"
    "s_waitcnt lgkmcnt(4)\n\t"
    "v_mfma_f32_16x16x32_bf16 v[4:7], v[52:55], v[208:211], v[4:7]\n\t"
    "v_cvt_pk_bf16_f32 v226, v232, v233\n\t"
    "v_cvt_pk_bf16_f32 v227, v234, v235\n\t"
    "v_mfma_f32_16x16x32_bf16 v[12:15], v[52:55], v[212:215], v[12:15]\n\t"
    "v_cvt_pk_bf16_f32 v228, v228, v229\n\t"
    "v_cvt_pk_bf16_f32 v229, v230, v231\n\t"
    "v_mfma_f32_16x16x32_bf16 v[8:11], v[48:51], v[212:215], v[8:11]\n\t"
    "v_cvt_pk_bf16_f32 v230, v236, v237\n\t"
    "v_cvt_pk_bf16_f32 v231, v238, v239\n\t"
    "s_waitcnt lgkmcnt(2)\n\t"
    "v_mfma_f32_16x16x32_bf16 v[0:3], v[56:59], v[224:227], v[0:3]\n\t"
    "s_waitcnt lgkmcnt(0)\n\t"
    "v_mfma_f32_16x16x32_bf16 v[4:7], v[60:63], v[224:227], v[4:7]\n\t"
    "v_mfma_f32_16x16x32_bf16 v[12:15], v[60:63], v[228:231], v[12:15]\n\t"
    "s_and_b32 s41, s60, 3\n\t"
    "s_mul_i32 s41, s41, s43\n\t"
    "v_mfma_f32_16x16x32_bf16 v[8:11], v[56:59], v[228:231], v[8:11]\n\t"
    "s_lshr_b32 s60, s60, 2\n\t"
    "s_waitcnt lgkmcnt(0)\n\t"
    "s_barrier\n\t"
    "ds_read_b128 v[200:203], v242 offset:0\n\t"
    "v_max3_f32 v16, v0, v1, v2\n\t"
    "s_nop 2\n\t"
    "v_max3_f32 v18, v8, v9, v10\n\t"
    "v_max3_f32 v17, v3, v4, v5\n\t"
    "v_max3_f32 v19, v11, v12, v13\n\t"
    "v_max3_f32 v16, v16, v6, v7\n\t"
    "v_max3_f32 v18, v18, v14, v15\n\t"
    "v_max_f32 v16, v16, v17\n\t"
    "ds_read_b128 v[204:207], v242 offset:2048\n\t"
    "v_max_f32 v18, v18, v19\n\t"
    "v_mov_b32 v17, v16\n\t"
    "v_mov_b32 v19, v18\n\t"
    "s_nop 0\n\t"
    "v_permlane16_swap_b32 v16, v17\n\t"
    "v_permlane16_swap_b32 v18, v19\n\t"
    "v_max_f32 v16, v16, v17\n\t"
    "v_max_f32 v18, v18, v19\n\t"
    "ds_read_b64_tr_b16 v[160:161], v240 offset:0\n\t"
    "v_mov_b32 v17, v16\n\t"
    "v_mov_b32 v19, v18\n\t"
    "s_nop 0\n\t"
    "v_permlane32_swap_b32 v16, v17\n\t"
    "v_permlane32_swap_b32 v18, v19\n\t"
    "v_max_f32 v16, v16, v17\n\t"
    "v_max_f32 v18, v18, v19\n\t"
    "v_mul_f32 v16, s49, v16\n\t"
    "ds_read_b64_tr_b16 v[162:163], v240 offset:1280\n\t"
    "v_mul_f32 v18, s49, v18\n\t"
    "v_fma_f32 v0, v0, s48, v16\n\t"
    "v_fma_f32 v8, v8, s48, v18\n\t"
    "v_fma_f32 v1, v1, s48, v16\n\t"
    "v_fma_f32 v9, v9, s48, v18\n\t"
    "v_fma_f32 v2, v2, s48, v16\n\t"
    "v_fma_f32 v10, v10, s48, v18\n\t"
    "ds_read_b64_tr_b16 v[164:165], v241 offset:0\n\t"
    "v_fma_f32 v3, v3, s48, v16\n\t"
    "v_fma_f32 v11, v11, s48, v18\n\t"
    "v_fma_f32 v4, v4, s48, v16\n\t"
    "v_fma_f32 v12, v12, s48, v18\n\t"
    "v_fma_f32 v5, v5, s48, v16\n\t"
    "v_fma_f32 v13, v13, s48, v18\n\t"
    "v_fma_f32 v6, v6, s48, v16\n\t"
    "ds_read_b64_tr_b16 v[166:167], v241 offset:1280\n\t"
    "v_fma_f32 v14, v14, s48, v18\n\t"
    "v_fma_f32 v7, v7, s48, v16\n\t"
    "v_fma_f32 v15, v15, s48, v18\n\t"
    "v_exp_f32 v0, v0\n\t"
    "v_exp_f32 v8, v8\n\t"
    "v_exp_f32 v1, v1\n\t"
    "v_exp_f32 v9, v9\n\t"
    "ds_read_b64_tr_b16 v[168:169], v240 offset:64\n\t"
    "v_exp_f32 v2, v2\n\t"
    "v_exp_f32 v10, v10\n\t"
    "v_exp_f32 v3, v3\n\t"
    "v_exp_f32 v11, v11\n\t"
    "v_exp_f32 v4, v4\n\t"
    "v_exp_f32 v12, v12\n\t"
    "v_exp_f32 v5, v5\n\t"
    "ds_read_b64_tr_b16 v[170:171], v240 offset:1344\n\t"
    "v_exp_f32 v13, v13\n\t"
    "v_exp_f32 v6, v6\n\t"
    "v_exp_f32 v14, v14\n\t"
    "v_exp_f32 v7, v7\n\t"
    "v_exp_f32 v15, v15\n\t"
    "v_add_f32 v16, v0, v1\n\t"
    "v_add_f32 v18, v8, v9\n\t"
    "ds_read_b64_tr_b16 v[172:173], v241 offset:64\n\t"
    "v_add_f32 v17, v2, v3\n\t"
    "v_add_f32 v19, v10, v11\n\t"
    "v_add_f32 v16, v16, v4\n\t"
    "v_add_f32 v18, v18, v12\n\t"
    "v_add_f32 v17, v17, v5\n\t"
    "v_add_f32 v19, v19, v13\n\t"
    "v_add_f32 v16, v16, v6\n\t"
    "ds_read_b64_tr_b16 v[174:175], v241 offset:1344\n\t"
    "v_add_f32 v18, v18, v14\n\t"
    "v_add_f32 v17, v17, v7\n\t"
    "v_add_f32 v19, v19, v15\n\t"
    "v_add_f32 v16, v16, v17\n\t"
    "v_add_f32 v18, v18, v19\n\t"
    "v_mov_b32 v17, v16\n\t"
    "v_mov_b32 v19, v18\n\t"
    "ds_read_b64_tr_b16 v[176:177], v240 offset:128\n\t"
    "v_permlane16_swap_b32 v16, v17\n\t"
    "v_permlane16_swap_b32 v18, v19\n\t"
    "v_add_f32 v16, v16, v17\n\t"
    "v_add_f32 v18, v18, v19\n\t"
    "v_mov_b32 v17, v16\n\t"
    "v_mov_b32 v19, v18\n\t"
    "s_nop 0\n\t"
    "v_permlane32_swap_b32 v16, v17\n\t"
    "ds_read_b64_tr_b16 v[178:179], v240 offset:1408\n\t"
    "v_permlane32_swap_b32 v18, v19\n\t"
    "v_add_f32 v16, v16, v17\n\t"
    "v_add_f32 v18, v18, v19\n\t"
    "v_rcp_f32 v16, v16\n\t"
    "v_rcp_f32 v18, v18\n\t"
    "v_mul_f32 v0, v0, v16\n\t"
    "v_mul_f32 v8, v8, v18\n\t"
    "ds_read_b64_tr_b16 v[180:181], v241 offset:128\n\t"
    "v_mul_f32 v1, v1, v16\n\t"
    "v_mul_f32 v9, v9, v18\n\t"
    "v_mul_f32 v2, v2, v16\n\t"
    "v_mul_f32 v10, v10, v18\n\t"
    "v_mul_f32 v3, v3, v16\n\t"
    "v_mul_f32 v11, v11, v18\n\t"
    "v_mul_f32 v4, v4, v16\n\t"
    "ds_read_b64_tr_b16 v[182:183], v241 offset:1408\n\t"
    "v_mul_f32 v12, v12, v18\n\t"
    "v_mul_f32 v5, v5, v16\n\t"
    "v_mul_f32 v13, v13, v18\n\t"
    "v_mul_f32 v6, v6, v16\n\t"
    "v_mul_f32 v14, v14, v18\n\t"
    "v_mul_f32 v7, v7, v16\n\t"
    "v_mul_f32 v15, v15, v18\n\t"
    "ds_read_b64_tr_b16 v[184:185], v240 offset:192\n\t"
    "v_cvt_pk_bf16_f32 v0, v0, v1\n\t"
    "v_cvt_pk_bf16_f32 v8, v8, v9\n\t"
    "v_cvt_pk_bf16_f32 v1, v2, v3\n\t"
    "v_cvt_pk_bf16_f32 v9, v10, v11\n\t"
    "v_cvt_pk_bf16_f32 v2, v4, v5\n\t"
    "v_cvt_pk_bf16_f32 v10, v12, v13\n\t"
    "v_cvt_pk_bf16_f32 v3, v6, v7\n\t"
    "ds_read_b64_tr_b16 v[186:187], v240 offset:1472\n\t"
    "v_cvt_pk_bf16_f32 v11, v14, v15\n\t"
    "buffer_store_dwordx4 v[0:3], v244, s[24:27], s62 offen sc1\n\t"
    "buffer_store_dwordx4 v[8:11], v244, s[24:27], s62 offen offset:1024 sc1\n\t"
    "s_waitcnt lgkmcnt(7)\n\t"
    "v_mfma_f32_16x16x32_bf16 v[80:83], v[200:203], v[160:163], 0\n\t"
    "ds_read_b64_tr_b16 v[188:189], v241 offset:192\n\t"
    "ds_read_b64_tr_b16 v[190:191], v241 offset:1472\n\t"
    "v_mfma_f32_16x16x32_bf16 v[84:87], v[204:207], v[160:163], 0\n\t"
    "ds_read_b64_tr_b16 v[192:193], v240 offset:256\n\t"
    "s_and_b32 s42, s60, 3\n\t"
    "v_mfma_f32_16x16x32_bf16 v[92:95], v[204:207], v[164:167], 0\n\t"
    "ds_read_b64_tr_b16 v[194:195], v240 offset:1536\n\t"
    "s_mul_i32 s42, s42, s43\n\t"
    "v_mfma_f32_16x16x32_bf16 v[88:91], v[200:203], v[164:167], 0\n\t"
    "ds_read_b64_tr_b16 v[196:197], v241 offset:256\n\t"
    "s_lshr_b32 s60, s60, 2\n\t"
    "v_mfma_f32_16x16x32_bf16 v[96:99], v[200:203], v[168:171], 0\n\t"
    "ds_read_b64_tr_b16 v[198:199], v241 offset:1536\n\t"
    "s_sub_u32 s45, s42, s41\n\t"
    "v_mfma_f32_16x16x32_bf16 v[100:103], v[204:207], v[168:171], 0\n\t"
    "ds_read_b128 v[208:211], v243 offset:0\n\t"
    "s_mov_b32 s41, s42\n\t"
    "s_waitcnt lgkmcnt(7)\n\t"
    "v_mfma_f32_16x16x32_bf16 v[108:111], v[204:207], v[172:175], 0\n\t"
    "ds_read_b128 v[212:215], v243 offset:2048\n\t"
    "v_add_u32 v242, s45, v242\n\t"
    "v_mfma_f32_16x16x32_bf16 v[104:107], v[200:203], v[172:175], 0\n\t"
    "ds_read_b64_tr_b16 v[160:161], v240 offset:10240\n\t"
    "v_add_u32 v243, s45, v243\n\t"
    "v_mfma_f32_16x16x32_bf16 v[112:115], v[200:203], v[176:179], 0\n\t"
    "ds_read_b64_tr_b16 v[162:163], v240 offset:11520\n\t"
    "v_mfma_f32_16x16x32_bf16 v[116:119], v[204:207], v[176:179], 0\n\t"
    "ds_read_b64_tr_b16 v[164:165], v241 offset:10240\n\t"
    "v_mfma_f32_16x16x32_bf16 v[124:127], v[204:207], v[180:183], 0\n\t"
    "ds_read_b64_tr_b16 v[166:167], v241 offset:11520\n\t"
    "v_mfma_f32_16x16x32_bf16 v[120:123], v[200:203], v[180:183], 0\n\t"
    "ds_read_b64_tr_b16 v[168:169], v240 offset:10304\n\t"
    "v_mfma_f32_16x16x32_bf16 v[128:131], v[200:203], v[184:187], 0\n\t"
    "ds_read_b64_tr_b16 v[170:171], v240 offset:11584\n\t"
    "v_mfma_f32_16x16x32_bf16 v[132:135], v[204:207], v[184:187], 0\n\t"
    "ds_read_b64_tr_b16 v[172:173], v241 offset:10304\n\t"
    "s_waitcnt lgkmcnt(7)\n\t"
    "v_mfma_f32_16x16x32_bf16 v[140:143], v[204:207], v[188:191], 0\n\t"
    "ds_read_b64_tr_b16 v[174:175], v241 offset:11584\n\t"
    "v_mfma_f32_16x16x32_bf16 v[136:139], v[200:203], v[188:191], 0\n\t"
    "ds_read_b64_tr_b16 v[176:177], v240 offset:10368\n\t"
    "v_mfma_f32_16x16x32_bf16 v[144:147], v[200:203], v[192:195], 0\n\t"
    "ds_read_b64_tr_b16 v[178:179], v240 offset:11648\n\t"
    "v_mfma_f32_16x16x32_bf16 v[148:151], v[204:207], v[192:195], 0\n\t"
    "ds_read_b64_tr_b16 v[180:181], v241 offset:10368\n\t"
    "v_mfma_f32_16x16x32_bf16 v[156:159], v[204:207], v[196:199], 0\n\t"
    "ds_read_b64_tr_b16 v[182:183], v241 offset:11648\n\t"
    "v_mfma_f32_16x16x32_bf16 v[152:155], v[200:203], v[196:199], 0\n\t"
    "ds_read_b64_tr_b16 v[184:185], v240 offset:10432\n\t"
    "s_waitcnt lgkmcnt(7)\n\t"
    "v_mfma_f32_16x16x32_bf16 v[80:83], v[208:211], v[160:163], v[80:83]\n\t"
    "ds_read_b64_tr_b16 v[186:187], v240 offset:11712\n\t"
    "v_mfma_f32_16x16x32_bf16 v[84:87], v[212:215], v[160:163], v[84:87]\n\t"
    "ds_read_b64_tr_b16 v[188:189], v241 offset:10432\n\t"
    "v_mfma_f32_16x16x32_bf16 v[92:95], v[212:215], v[164:167], v[92:95]\n\t"
    "ds_read_b64_tr_b16 v[190:191], v241 offset:11712\n\t"
    "v_mfma_f32_16x16x32_bf16 v[88:91], v[208:211], v[164:167], v[88:91]\n\t"
    "ds_read_b64_tr_b16 v[192:193], v240 offset:10496\n\t"
    "v_mfma_f32_16x16x32_bf16 v[96:99], v[208:211], v[168:171], v[96:99]\n\t"
    "ds_read_b64_tr_b16 v[194:195], v240 offset:11776\n\t"
    "v_add_u32 v240, s45, v240\n\t"
    "v_mfma_f32_16x16x32_bf16 v[100:103], v[212:215], v[168:171], v[100:103]\n\t"
    "ds_read_b64_tr_b16 v[196:197], v241 offset:10496\n\t"
    "s_waitcnt lgkmcnt(7)\n\t"
    "v_mfma_f32_16x16x32_bf16 v[108:111], v[212:215], v[172:175], v[108:111]\n\t"
    "ds_read_b64_tr_b16 v[198:199], v241 offset:11776\n\t"
    "v_add_u32 v241, s45, v241\n\t"
    "v_mfma_f32_16x16x32_bf16 v[104:107], v[208:211], v[172:175], v[104:107]\n\t"
    "v_mfma_f32_16x16x32_bf16 v[112:115], v[208:211], v[176:179], v[112:115]\n\t"
    "v_mfma_f32_16x16x32_bf16 v[116:119], v[212:215], v[176:179], v[116:119]\n\t"
    "s_waitcnt lgkmcnt(0)\n\t"
    "s_barrier\n\t"
    "v_mfma_f32_16x16x32_bf16 v[124:127], v[212:215], v[180:183], v[124:127]\n\t"
    "ds_read_b128 v[200:203], v242 offset:0\n\t"
    "ds_read_b128 v[204:207], v242 offset:2048\n\t"
    "v_mfma_f32_16x16x32_bf16 v[120:123], v[208:211], v[180:183], v[120:123]\n\t"
    "ds_read_b64_tr_b16 v[160:161], v240 offset:0\n\t"
    "ds_read_b64_tr_b16 v[162:163], v240 offset:1280\n\t"
    "v_mfma_f32_16x16x32_bf16 v[128:131], v[208:211], v[184:187], v[128:131]\n\t"
    "ds_read_b64_tr_b16 v[164:165], v241 offset:0\n\t"
    "ds_read_b64_tr_b16 v[166:167], v241 offset:1280\n\t"
    "v_mfma_f32_16x16x32_bf16 v[132:135], v[212:215], v[184:187], v[132:135]\n\t"
    "ds_read_b64_tr_b16 v[168:169], v240 offset:64\n\t"
    "ds_read_b64_tr_b16 v[170:171], v240 offset:1344\n\t"
    "v_mfma_f32_16x16x32_bf16 v[140:143], v[212:215], v[188:191], v[140:143]\n\t"
    "ds_read_b64_tr_b16 v[172:173], v241 offset:64\n\t"
    "ds_read_b64_tr_b16 v[174:175], v241 offset:1344\n\t"
    "v_mfma_f32_16x16x32_bf16 v[136:139], v[208:211], v[188:191], v[136:139]\n\t"
    "ds_read_b64_tr_b16 v[176:177], v240 offset:128\n\t"
    "ds_read_b64_tr_b16 v[178:179], v240 offset:1408\n\t"
    "v_mfma_f32_16x16x32_bf16 v[144:147], v[208:211], v[192:195], v[144:147]\n\t"
    "ds_read_b64_tr_b16 v[180:181], v241 offset:128\n\t"
    "ds_read_b64_tr_b16 v[182:183], v241 offset:1408\n\t"
    "v_mfma_f32_16x16x32_bf16 v[148:151], v[212:215], v[192:195], v[148:151]\n\t"
    "ds_read_b64_tr_b16 v[184:185], v240 offset:192\n\t"
    "s_and_b32 s42, s60, 3\n\t"
    "v_mfma_f32_16x16x32_bf16 v[156:159], v[212:215], v[196:199], v[156:159]\n\t"
    "ds_read_b64_tr_b16 v[186:187], v240 offset:1472\n\t"
    "s_mul_i32 s42, s42, s43\n\t"
    "v_mfma_f32_16x16x32_bf16 v[152:155], v[208:211], v[196:199], v[152:155]\n\t"
    "ds_read_b64_tr_b16 v[188:189], v241 offset:192\n\t"
    "s_lshr_b32 s60, s60, 2\n\t"
    "s_waitcnt lgkmcnt(7)\n\t"
    "v_mfma_f32_16x16x32_bf16 v[80:83], v[200:203], v[160:163], v[80:83]\n\t"
    "ds_read_b64_tr_b16 v[190:191], v241 offset:1472\n\t"
    "s_sub_u32 s45, s42, s41\n\t"
    "v_mfma_f32_16x16x32_bf16 v[84:87], v[204:207], v[160:163], v[84:87]\n\t"
    "ds_read_b64_tr_b16 v[192:193], v240 offset:256\n\t"
    "s_mov_b32 s41, s42\n\t"
    "v_mfma_f32_16x16x32_bf16 v[92:95], v[204:207], v[164:167], v[92:95]\n\t"
    "ds_read_b64_tr_b16 v[194:195], v240 offset:1536\n\t"
    "v_add_u32 v242, s45, v242\n\t"
    "v_mfma_f32_16x16x32_bf16 v[88:91], v[200:203], v[164:167], v[88:91]\n\t"
    "ds_read_b64_tr_b16 v[196:197], v241 offset:256\n\t"
    "s_waitcnt vmcnt(2)\n\t"
    "s_mov_b32 s44, 0x9e3779b1\n\t"
    "v_mfma_f32_16x16x32_bf16 v[96:99], v[200:203], v[168:171], v[96:99]\n\t"
    "ds_read_b64_tr_b16 v[198:199], v241 offset:1536\n\t"
    "s_mov_b32 s46, 0x85ebca77\n\t"
    "v_mfma_f32_16x16x32_bf16 v[100:103], v[204:207], v[168:171], v[100:103]\n\t"
    "ds_read_b128 v[208:211], v243 offset:0\n\t"
    "v_mul_lo_u32 v248, v248, s44\n\t"
    "v_mfma_f32_16x16x32_bf16 v[108:111], v[204:207], v[172:175], v[108:111]\n\t"
    "ds_read_b128 v[212:215], v243 offset:2048\n\t"
    "v_add_u32 v243, s45, v243\n\t"
    "v_mfma_f32_16x16x32_bf16 v[104:107], v[200:203], v[172:175], v[104:107]\n\t"
    "ds_read_b64_tr_b16 v[160:161], v240 offset:10240\n\t"
    "v_mul_lo_u32 v249, v249, s46\n\t"
    "s_waitcnt lgkmcnt(7)\n\t"
    "v_mfma_f32_16x16x32_bf16 v[112:115], v[200:203], v[176:179], v[112:115]\n\t"
    "ds_read_b64_tr_b16 v[162:163], v240 offset:11520\n\t"
    "v_add_u32 v248, 0x632be5ab, v248\n\t"
    "v_mfma_f32_16x16x32_bf16 v[116:119], v[204:207], v[176:179], v[116:119]\n\t"
    "ds_read_b64_tr_b16 v[164:165], v241 offset:10240\n\t"
    "v_add_u32 v249, 0xd9b4e019, v249\n\t"
    "v_mfma_f32_16x16x32_bf16 v[124:127], v[204:207], v[180:183], v[124:127]\n\t"
    "ds_read_b64_tr_b16 v[166:167], v241 offset:11520\n\t"
    "s_nop 0\n\t"
    "v_mfma_f32_16x16x32_bf16 v[120:123], v[200:203], v[180:183], v[120:123]\n\t"
    "ds_read_b64_tr_b16 v[168:169], v240 offset:10304\n\t"
    "v_readlane_b32 s54, v248, 8\n\t"
    "v_mfma_f32_16x16x32_bf16 v[128:131], v[200:203], v[184:187], v[128:131]\n\t"
    "ds_read_b64_tr_b16 v[170:171], v240 offset:11584\n\t"
    "v_readlane_b32 s55, v249, 8\n\t"
    "v_mfma_f32_16x16x32_bf16 v[132:135], v[204:207], v[184:187], v[132:135]\n\t"
    "ds_read_b64_tr_b16 v[172:173], v241 offset:10304\n\t"
    "v_mfma_f32_16x16x32_bf16 v[140:143], v[204:207], v[188:191], v[140:143]\n\t"
    "ds_read_b64_tr_b16 v[174:175], v241 offset:11584\n\t"
    "v_mfma_f32_16x16x32_bf16 v[136:139], v[200:203], v[188:191], v[136:139]\n\t"
    "ds_read_b64_tr_b16 v[176:177], v240 offset:10368\n\t"
    "s_waitcnt lgkmcnt(7)\n\t"
    "v_mfma_f32_16x16x32_bf16 v[144:147], v[200:203], v[192:195], v[144:147]\n\t"
    "ds_read_b64_tr_b16 v[178:179], v240 offset:11648\n\t"
    "v_mfma_f32_16x16x32_bf16 v[148:151], v[204:207], v[192:195], v[148:151]\n\t"
    "ds_read_b64_tr_b16 v[180:181], v241 offset:10368\n\t"
    "v_mfma_f32_16x16x32_bf16 v[156:159], v[204:207], v[196:199], v[156:159]\n\t"
    "ds_read_b64_tr_b16 v[182:183], v241 offset:11648\n\t"
    "v_mfma_f32_16x16x32_bf16 v[152:155], v[200:203], v[196:199], v[152:155]\n\t"
    "ds_read_b64_tr_b16 v[184:185], v240 offset:10432\n\t"
    "v_mfma_f32_16x16x32_bf16 v[80:83], v[208:211], v[160:163], v[80:83]\n\t"
    "ds_read_b64_tr_b16 v[186:187], v240 offset:11712\n\t"
    "s_waitcnt vmcnt(0)\n\t"
    "s_mov_b64 exec, 15\n\t"
    "buffer_store_dwordx2 v[248:249], v254, s[24:27], s65 offen sc1\n\t"
    "s_mov_b64 exec, -1\n\t"
    "v_mfma_f32_16x16x32_bf16 v[84:87], v[212:215], v[160:163], v[84:87]\n\t"
    "ds_read_b64_tr_b16 v[188:189], v241 offset:10432\n\t"
    "v_lshlrev_b32 v254, 6, v254\n\t"
    "s_waitcnt lgkmcnt(7)\n\t"
    "v_mfma_f32_16x16x32_bf16 v[92:95], v[212:215], v[164:167], v[92:95]\n\t"
    "ds_read_b64_tr_b16 v[190:191], v241 offset:11712\n\t"
    "s_mov_b32 s40, 1\n\t"
    "v_mfma_f32_16x16x32_bf16 v[88:91], v[208:211], v[164:167], v[88:91]\n\t"
    "ds_read_b64_tr_b16 v[192:193], v240 offset:10496\n\t"
    "v_mfma_f32_16x16x32_bf16 v[96:99], v[208:211], v[168:171], v[96:99]\n\t"
    "ds_read_b64_tr_b16 v[194:195], v240 offset:11776\n\t"
    "v_add_u32 v240, s45, v240\n\t"
    "v_mfma_f32_16x16x32_bf16 v[100:103], v[212:215], v[168:171], v[100:103]\n\t"
    "ds_read_b64_tr_b16 v[196:197], v241 offset:10496\n\t"
    "v_mfma_f32_16x16x32_bf16 v[108:111], v[212:215], v[172:175], v[108:111]\n\t"
    "ds_read_b64_tr_b16 v[198:199], v241 offset:11776\n\t"
    "v_add_u32 v241, s45, v241\n\t"
    "v_mfma_f32_16x16x32_bf16 v[104:107], v[208:211], v[172:175], v[104:107]\n\t"
    "s_waitcnt lgkmcnt(7)\n\t"
    "v_mfma_f32_16x16x32_bf16 v[112:115], v[208:211], v[176:179], v[112:115]\n\t"
    "v_mfma_f32_16x16x32_bf16 v[116:119], v[212:215], v[176:179], v[116:119]\n\t"
    "s_waitcnt lgkmcnt(0)\n\t"
    "s_barrier\n\t"
    "v_mfma_f32_16x16x32_bf16 v[124:127], v[212:215], v[180:183], v[124:127]\n\t"
    "ds_read_b128 v[200:203], v242 offset:0\n\t"
    "ds_read_b128 v[204:207], v242 offset:2048\n\t"
    "v_mfma_f32_16x16x32_bf16 v[120:123], v[208:211], v[180:183], v[120:123]\n\t"
    "ds_read_b64_tr_b16 v[160:161], v240 offset:0\n\t"
    "ds_read_b64_tr_b16 v[162:163], v240 offset:1280\n\t"
    "v_mfma_f32_16x16x32_bf16 v[128:131], v[208:211], v[184:187], v[128:131]\n\t"
    "ds_read_b64_tr_b16 v[164:165], v241 offset:0\n\t"
    "ds_read_b64_tr_b16 v[166:167], v241 offset:1280\n\t"
    "v_mfma_f32_16x16x32_bf16 v[132:135], v[212:215], v[184:187], v[132:135]\n\t"
    "ds_read_b64_tr_b16 v[168:169], v240 offset:64\n\t"
    "ds_read_b64_tr_b16 v[170:171], v240 offset:1344\n\t"
    "v_mfma_f32_16x16x32_bf16 v[140:143], v[212:215], v[188:191], v[140:143]\n\t"
    "ds_read_b64_tr_b16 v[172:173], v241 offset:64\n\t"
    "ds_read_b64_tr_b16 v[174:175], v241 offset:1344\n\t"
    "v_mfma_f32_16x16x32_bf16 v[136:139], v[208:211], v[188:191], v[136:139]\n\t"
    "ds_read_b64_tr_b16 v[176:177], v240 offset:128\n\t"
    "ds_read_b64_tr_b16 v[178:179], v240 offset:1408\n\t"
    "v_mfma_f32_16x16x32_bf16 v[144:147], v[208:211], v[192:195], v[144:147]\n\t"
    "ds_read_b64_tr_b16 v[180:181], v240 offset:10240\n\t"
    "ds_read_b64_tr_b16 v[182:183], v240 offset:11520\n\t"
    "v_mfma_f32_16x16x32_bf16 v[148:151], v[212:215], v[192:195], v[148:151]\n\t"
    "ds_read_b64_tr_b16 v[184:185], v241 offset:10240\n\t"
    "s_and_b32 s42, s60, 3\n\t"
    "v_mfma_f32_16x16x32_bf16 v[156:159], v[212:215], v[196:199], v[156:159]\n\t"
    "ds_read_b64_tr_b16 v[186:187], v241 offset:11520\n\t"
    "s_mul_i32 s42, s42, s43\n\t"
    "v_mfma_f32_16x16x32_bf16 v[152:155], v[208:211], v[196:199], v[152:155]\n\t"
    "ds_read_b64_tr_b16 v[188:189], v240 offset:10304\n\t"
    "s_lshr_b32 s60, s60, 2\n\t"
    "s_waitcnt lgkmcnt(7)\n\t"
    "v_mfma_f32_16x16x32_bf16 v[80:83], v[200:203], v[160:163], v[80:83]\n\t"
    "ds_read_b64_tr_b16 v[190:191], v240 offset:11584\n\t"
    "s_sub_u32 s45, s42, s41\n\t"
    "v_mfma_f32_16x16x32_bf16 v[84:87], v[204:207], v[160:163], v[84:87]\n\t"
    "ds_read_b64_tr_b16 v[192:193], v241 offset:10304\n\t"
    "s_mov_b32 s41, s42\n\t"
    "v_mfma_f32_16x16x32_bf16 v[92:95], v[204:207], v[164:167], v[92:95]\n\t"
    "ds_read_b64_tr_b16 v[194:195], v241 offset:11584\n\t"
    "v_add_u32 v242, s45, v242\n\t"
    "v_mfma_f32_16x16x32_bf16 v[88:91], v[200:203], v[164:167], v[88:91]\n\t"
    "ds_read_b128 v[208:211], v243 offset:0\n\t"
    "v_mfma_f32_16x16x32_bf16 v[96:99], v[200:203], v[168:171], v[96:99]\n\t"
    "ds_read_b128 v[212:215], v243 offset:2048\n\t"
    "v_add_u32 v243, s45, v243\n\t"
    "v_mfma_f32_16x16x32_bf16 v[100:103], v[204:207], v[168:171], v[100:103]\n\t"
    "ds_read_b64_tr_b16 v[196:197], v240 offset:10368\n\t"
    "v_mfma_f32_16x16x32_bf16 v[108:111], v[204:207], v[172:175], v[108:111]\n\t"
    "ds_read_b64_tr_b16 v[198:199], v240 offset:11648\n\t"
    "v_mfma_f32_16x16x32_bf16 v[104:107], v[200:203], v[172:175], v[104:107]\n\t"
    "ds_read_b64_tr_b16 v[160:161], v241 offset:128\n\t"
    "s_waitcnt lgkmcnt(7)\n\t"
    "v_mfma_f32_16x16x32_bf16 v[112:115], v[200:203], v[176:179], v[112:115]\n\t"
    "ds_read_b64_tr_b16 v[162:163], v241 offset:1408\n\t"
    "v_mfma_f32_16x16x32_bf16 v[116:119], v[204:207], v[176:179], v[116:119]\n\t"
    "ds_read_b64_tr_b16 v[164:165], v240 offset:192\n\t"
    "s_waitcnt lgkmcnt(5)\n\t"
    "v_mfma_f32_16x16x32_bf16 v[84:87], v[212:215], v[180:183], v[84:87]\n\t"
    "ds_read_b64_tr_b16 v[166:167], v240 offset:1472\n\t"
    "v_mfma_f32_16x16x32_bf16 v[80:83], v[208:211], v[180:183], v[80:83]\n\t"
    "ds_read_b64_tr_b16 v[168:169], v241 offset:192\n\t"
    "v_mfma_f32_16x16x32_bf16 v[88:91], v[208:211], v[184:187], v[88:91]\n\t"
    "ds_read_b64_tr_b16 v[170:171], v241 offset:1472\n\t"
    "v_mfma_f32_16x16x32_bf16 v[92:95], v[212:215], v[184:187], v[92:95]\n\t"
    "ds_read_b64_tr_b16 v[172:173], v240 offset:256\n\t"
    "v_mfma_f32_16x16x32_bf16 v[100:103], v[212:215], v[188:191], v[100:103]\n\t"
    "ds_read_b64_tr_b16 v[174:175], v240 offset:1536\n\t"
    "v_mfma_f32_16x16x32_bf16 v[96:99], v[208:211], v[188:191], v[96:99]\n\t"
    "ds_read_b64_tr_b16 v[176:177], v241 offset:256\n\t"
    "v_cvt_pk_bf16_f32 v0, v80, v81\n\t"
    "v_mfma_f32_16x16x32_bf16 v[104:107], v[208:211], v[192:195], v[104:107]\n\t"
    "ds_read_b64_tr_b16 v[178:179], v241 offset:1536\n\t"
    "v_cvt_pk_bf16_f32 v1, v82, v83\n\t"
    "v_mfma_f32_16x16x32_bf16 v[108:111], v[212:215], v[192:195], v[108:111]\n\t"
    "ds_read_b64_tr_b16 v[180:181], v241 offset:10368\n\t"
    "v_cvt_pk_bf16_f32 v2, v84, v85\n\t"
    "s_waitcnt lgkmcnt(7)\n\t"
    "v_mfma_f32_16x16x32_bf16 v[116:119], v[212:215], v[196:199], v[116:119]\n\t"
    "ds_read_b64_tr_b16 v[182:183], v241 offset:11648\n\t"
    "v_cvt_pk_bf16_f32 v3, v86, v87\n\t"
    "v_mfma_f32_16x16x32_bf16 v[112:115], v[208:211], v[196:199], v[112:115]\n\t"
    "ds_read_b64_tr_b16 v[184:185], v240 offset:10432\n\t"
    "v_cvt_pk_bf16_f32 v8, v88, v89\n\t"
    "v_mfma_f32_16x16x32_bf16 v[120:123], v[200:203], v[160:163], v[120:123]\n\t"
    "ds_read_b64_tr_b16 v[186:187], v240 offset:11712\n\t"
    "v_cvt_pk_bf16_f32 v9, v90, v91\n\t"
    "v_mfma_f32_16x16x32_bf16 v[124:127], v[204:207], v[160:163], v[124:127]\n\t"
    "ds_read_b64_tr_b16 v[188:189], v241 offset:10432\n\t"
    "v_cvt_pk_bf16_f32 v10, v92, v93\n\t"
    "v_mfma_f32_16x16x32_bf16 v[132:135], v[204:207], v[164:167], v[132:135]\n\t"
    "ds_read_b64_tr_b16 v[190:191], v241 offset:11712\n\t"
    "v_cvt_pk_bf16_f32 v11, v94, v95\n\t"
    "v_mfma_f32_16x16x32_bf16 v[128:131], v[200:203], v[164:167], v[128:131]\n\t"
    "ds_read_b64_tr_b16 v[192:193], v240 offset:10496\n\t"
    "v_cvt_pk_bf16_f32 v16, v96, v97\n\t"
    "s_waitcnt lgkmcnt(7)\n\t"
    "v_mfma_f32_16x16x32_bf16 v[136:139], v[200:203], v[168:171], v[136:139]\n\t"
    "ds_read_b64_tr_b16 v[194:195], v240 offset:11776\n\t"
    "v_add_u32 v240, s45, v240\n\t"
    "v_mfma_f32_16x16x32_bf16 v[140:143], v[204:207], v[168:171], v[140:143]\n\t"
    "ds_read_b64_tr_b16 v[196:197], v241 offset:10496\n\t"
    "v_cvt_pk_bf16_f32 v17, v98, v99\n\t"
    "v_mfma_f32_16x16x32_bf16 v[148:151], v[204:207], v[172:175], v[148:151]\n\t"
    "ds_read_b64_tr_b16 v[198:199], v241 offset:11776\n\t"
    "v_add_u32 v241, s45, v241\n\t"
    "v_mfma_f32_16x16x32_bf16 v[144:147], v[200:203], v[172:175], v[144:147]\n\t"
    "v_cvt_pk_bf16_f32 v18, v100, v101\n\t"
    "v_cvt_pk_bf16_f32 v19, v102, v103\n\t"
    "v_mfma_f32_16x16x32_bf16 v[152:155], v[200:203], v[176:179], v[152:155]\n\t"
    "v_cvt_pk_bf16_f32 v24, v104, v105\n\t"
    "v_cvt_pk_bf16_f32 v25, v106, v107\n\t"
    "v_mfma_f32_16x16x32_bf16 v[156:159], v[204:207], v[176:179], v[156:159]\n\t"
    "v_cvt_pk_bf16_f32 v26, v108, v109\n\t"
    "v_cvt_pk_bf16_f32 v27, v110, v111\n\t"
    "s_waitcnt lgkmcnt(0)\n\t"
    "s_barrier\n\t"
    "v_mfma_f32_16x16x32_bf16 v[124:127], v[212:215], v[180:183], v[124:127]\n\t"
    "ds_read_b128 v[200:203], v242 offset:0\n\t"
    "v_cvt_pk_bf16_f32 v32, v112, v113\n\t"
    "v_mfma_f32_16x16x32_bf16 v[120:123], v[208:211], v[180:183], v[120:123]\n\t"
    "ds_read_b128 v[204:207], v242 offset:2048\n\t"
    "v_cvt_pk_bf16_f32 v33, v114, v115\n\t"
    "v_mfma_f32_16x16x32_bf16 v[128:131], v[208:211], v[184:187], v[128:131]\n\t"
    "ds_read_b64_tr_b16 v[160:161], v240 offset:0\n\t"
    "v_cvt_pk_bf16_f32 v34, v116, v117\n\t"
    "v_mfma_f32_16x16x32_bf16 v[132:135], v[212:215], v[184:187], v[132:135]\n\t"
    "ds_read_b64_tr_b16 v[162:163], v240 offset:1280\n\t"
    "v_cvt_pk_bf16_f32 v35, v118, v119\n\t"
    "v_mfma_f32_16x16x32_bf16 v[140:143], v[212:215], v[188:191], v[140:143]\n\t"
    "ds_read_b64_tr_b16 v[164:165], v241 offset:0\n\t"
    "v_mfma_f32_16x16x32_bf16 v[136:139], v[208:211], v[188:191], v[136:139]\n\t"
    "ds_read_b64_tr_b16 v[166:167], v241 offset:1280\n\t"
    "v_cvt_pk_bf16_f32 v40, v120, v121\n\t"
    "v_mfma_f32_16x16x32_bf16 v[144:147], v[208:211], v[192:195], v[144:147]\n\t"
    "ds_read_b64_tr_b16 v[168:169], v240 offset:64\n\t"
    "v_cvt_pk_bf16_f32 v41, v122, v123\n\t"
    "v_mfma_f32_16x16x32_bf16 v[148:151], v[212:215], v[192:195], v[148:151]\n\t"
    "ds_read_b64_tr_b16 v[170:171], v240 offset:1344\n\t"
    "v_cvt_pk_bf16_f32 v42, v124, v125\n\t"
    "v_mfma_f32_16x16x32_bf16 v[156:159], v[212:215], v[196:199], v[156:159]\n\t"
    "ds_read_b64_tr_b16 v[172:173], v241 offset:64\n\t"
    "v_cvt_pk_bf16_f32 v43, v126, v127\n\t"
    "v_mfma_f32_16x16x32_bf16 v[152:155], v[208:211], v[196:199], v[152:155]\n\t"
    "ds_read_b64_tr_b16 v[174:175], v241 offset:1344\n\t"
    "v_cvt_pk_bf16_f32 v48, v128, v129\n\t"
    "ds_read_b64_tr_b16 v[176:177], v240 offset:128\n\t"
    "v_cvt_pk_bf16_f32 v49, v130, v131\n\t"
    "v_cvt_pk_bf16_f32 v50, v132, v133\n\t"
    "ds_read_b64_tr_b16 v[178:179], v240 offset:1408\n\t"
    "v_cvt_pk_bf16_f32 v51, v134, v135\n\t"
    "v_cvt_pk_bf16_f32 v56, v136, v137\n\t"
    "ds_read_b64_tr_b16 v[180:181], v241 offset:128\n\t"
    "v_cvt_pk_bf16_f32 v57, v138, v139\n\t"
    "v_cvt_pk_bf16_f32 v58, v140, v141\n\t"
    "ds_read_b64_tr_b16 v[182:183], v241 offset:1408\n\t"
    "v_cvt_pk_bf16_f32 v59, v142, v143\n\t"
    "v_cvt_pk_bf16_f32 v64, v144, v145\n\t"
    "ds_read_b64_tr_b16 v[184:185], v240 offset:192\n\t"
    "v_cvt_pk_bf16_f32 v65, v146, v147\n\t"
    "v_cvt_pk_bf16_f32 v66, v148, v149\n\t"
    "ds_read_b64_tr_b16 v[186:187], v240 offset:1472\n\t"
    "v_cvt_pk_bf16_f32 v67, v150, v151\n\t"
    "v_cvt_pk_bf16_f32 v72, v152, v153\n\t"
    "v_cvt_pk_bf16_f32 v73, v154, v155\n\t"
    "v_cvt_pk_bf16_f32 v74, v156, v157\n\t"
    "v_cvt_pk_bf16_f32 v75, v158, v159\n\t"
    "s_nop 1\n\t"
    "s_branch COM%=\n\t"
    "HB%=:\n\t"
    "v_mfma_f32_16x16x32_bf16 v[124:127], v[212:215], v[180:183], v[124:127]\n\t"
    "ds_read_b128 v[224:227], v245 offset:0\n\t"
    "v_cvt_pk_bf16_f32 v112, v112, v113\n\t"
    "v_mfma_f32_16x16x32_bf16 v[120:123], v[208:211], v[180:183], v[120:123]\n\t"
    "ds_read_b128 v[228:231], v245 offset:1024\n\t"
    "v_cvt_pk_bf16_f32 v113, v114, v115\n\t"
    "v_mfma_f32_16x16x32_bf16 v[128:131], v[208:211], v[184:187], v[128:131]\n\t"
    "ds_read_b128 v[216:219], v245 offset:2048\n\t"
    "v_cvt_pk_bf16_f32 v114, v116, v117\n\t"
    "v_mfma_f32_16x16x32_bf16 v[132:135], v[212:215], v[184:187], v[132:135]\n\t"
    "ds_read_b128 v[220:223], v245 offset:3072\n\t"
    "v_cvt_pk_bf16_f32 v115, v118, v119\n\t"
    "v_mfma_f32_16x16x32_bf16 v[140:143], v[212:215], v[188:191], v[140:143]\n\t"
    "v_mfma_f32_16x16x32_bf16 v[136:139], v[208:211], v[188:191], v[136:139]\n\t"
    "v_cvt_pk_bf16_f32 v120, v120, v121\n\t"
    "v_mfma_f32_16x16x32_bf16 v[144:147], v[208:211], v[192:195], v[144:147]\n\t"
    "v_cvt_pk_bf16_f32 v121, v122, v123\n\t"
    "v_mfma_f32_16x16x32_bf16 v[148:151], v[212:215], v[192:195], v[148:151]\n\t"
    "v_cvt_pk_bf16_f32 v122, v124, v125\n\t"
    "v_mfma_f32_16x16x32_bf16 v[156:159], v[212:215], v[196:199], v[156:159]\n\t"
    "v_cvt_pk_bf16_f32 v123, v126, v127\n\t"
    "v_mfma_f32_16x16x32_bf16 v[152:155], v[208:211], v[196:199], v[152:155]\n\t"
    "s_waitcnt lgkmcnt(0)\n\t"
    "v_cvt_pk_bf16_f32 v128, v128, v129\n\t"
    "v_mfma_f32_16x16x32_bf16 v[4:7], v[0:3], v[228:231], 0\n\t"
    "v_cvt_pk_bf16_f32 v129, v130, v131\n\t"
    "v_mfma_f32_16x16x32_bf16 v[0:3], v[0:3], v[224:227], 0\n\t"
    "v_cvt_pk_bf16_f32 v130, v132, v133\n\t"
    "v_mfma_f32_16x16x32_bf16 v[12:15], v[8:11], v[228:231], 0\n\t"
    "v_cvt_pk_bf16_f32 v131, v134, v135\n\t"
    "v_mfma_f32_16x16x32_bf16 v[8:11], v[8:11], v[224:227], 0\n\t"
    "v_cvt_pk_bf16_f32 v136, v136, v137\n\t"
    "v_mfma_f32_16x16x32_bf16 v[20:23], v[16:19], v[228:231], 0\n\t"
    "v_cvt_pk_bf16_f32 v137, v138, v139\n\t"
    "v_mfma_f32_16x16x32_bf16 v[16:19], v[16:19], v[224:227], 0\n\t"
    "v_cvt_pk_bf16_f32 v138, v140, v141\n\t"
    "v_mfma_f32_16x16x32_bf16 v[28:31], v[24:27], v[228:231], 0\n\t"
    "v_cvt_pk_bf16_f32 v139, v142, v143\n\t"
    "v_mfma_f32_16x16x32_bf16 v[24:27], v[24:27], v[224:227], 0\n\t"
    "v_cvt_pk_bf16_f32 v144, v144, v145\n\t"
    "v_mfma_f32_16x16x32_bf16 v[36:39], v[32:35], v[228:231], 0\n\t"
    "v_cvt_pk_bf16_f32 v145, v146, v147\n\t"
    "v_mfma_f32_16x16x32_bf16 v[32:35], v[32:35], v[224:227], 0\n\t"
    "v_cvt_pk_bf16_f32 v146, v148, v149\n\t"
    "v_mfma_f32_16x16x32_bf16 v[44:47], v[40:43], v[228:231], 0\n\t"
    "v_cvt_pk_bf16_f32 v147, v150, v151\n\t"
    "v_mfma_f32_16x16x32_bf16 v[40:43], v[40:43], v[224:227], 0\n\t"
    "v_cvt_pk_bf16_f32 v152, v152, v153\n\t"
    "v_mfma_f32_16x16x32_bf16 v[52:55], v[48:51], v[228:231], 0\n\t"
    "v_cvt_pk_bf16_f32 v153, v154, v155\n\t"
    "v_mfma_f32_16x16x32_bf16 v[48:51], v[48:51], v[224:227], 0\n\t"
    "v_cvt_pk_bf16_f32 v154, v156, v157\n\t"
    "v_mfma_f32_16x16x32_bf16 v[60:63], v[56:59], v[228:231], 0\n\t"
    "v_cvt_pk_bf16_f32 v155, v158, v159\n\t"
    "v_mfma_f32_16x16x32_bf16 v[56:59], v[56:59], v[224:227], 0\n\t"
    "v_mfma_f32_16x16x32_bf16 v[68:71], v[64:67], v[228:231], 0\n\t"
    "v_mfma_f32_16x16x32_bf16 v[64:67], v[64:67], v[224:227], 0\n\t"
    "v_mfma_f32_16x16x32_bf16 v[76:79], v[72:75], v[228:231], 0\n\t"
    "ds_read_b128 v[200:203], v242 offset:0\n\t"
    "v_mfma_f32_16x16x32_bf16 v[72:75], v[72:75], v[224:227], 0\n\t"
    "ds_read_b128 v[204:207], v242 offset:2048\n\t"
    "v_mfma_f32_16x16x32_bf16 v[0:3], v[80:83], v[216:219], v[0:3]\n\t"
    "ds_read_b64_tr_b16 v[160:161], v240 offset:0\n\t"
    "v_mfma_f32_16x16x32_bf16 v[4:7], v[80:83], v[220:223], v[4:7]\n\t"
    "ds_read_b64_tr_b16 v[162:163], v240 offset:1280\n\t"
    "v_mfma_f32_16x16x32_bf16 v[12:15], v[88:91], v[220:223], v[12:15]\n\t"
    "ds_read_b64_tr_b16 v[164:165], v241 offset:0\n\t"
    "v_mfma_f32_16x16x32_bf16 v[8:11], v[88:91], v[216:219], v[8:11]\n\t"
    "ds_read_b64_tr_b16 v[166:167], v241 offset:1280\n\t"
    "v_mfma_f32_16x16x32_bf16 v[16:19], v[96:99], v[216:219], v[16:19]\n\t"
    "ds_read_b64_tr_b16 v[168:169], v240 offset:64\n\t"
    "v_mfma_f32_16x16x32_bf16 v[20:23], v[96:99], v[220:223], v[20:23]\n\t"
    "ds_read_b64_tr_b16 v[170:171], v240 offset:1344\n\t"
    "v_mfma_f32_16x16x32_bf16 v[28:31], v[104:107], v[220:223], v[28:31]\n\t"
    "ds_read_b64_tr_b16 v[172:173], v241 offset:64\n\t"
    "v_mfma_f32_16x16x32_bf16 v[24:27], v[104:107], v[216:219], v[24:27]\n\t"
    "ds_read_b64_tr_b16 v[174:175], v241 offset:1344\n\t"
    "v_mfma_f32_16x16x32_bf16 v[32:35], v[112:115], v[216:219], v[32:35]\n\t"
    "ds_read_b64_tr_b16 v[176:177], v240 offset:128\n\t"
    "v_mfma_f32_16x16x32_bf16 v[36:39], v[112:115], v[220:223], v[36:39]\n\t"
    "ds_read_b64_tr_b16 v[178:179], v240 offset:1408\n\t"
    "v_mfma_f32_16x16x32_bf16 v[44:47], v[120:123], v[220:223], v[44:47]\n\t"
    "ds_read_b64_tr_b16 v[180:181], v241 offset:128\n\t"
    "v_mfma_f32_16x16x32_bf16 v[40:43], v[120:123], v[216:219], v[40:43]\n\t"
    "ds_read_b64_tr_b16 v[182:183], v241 offset:1408\n\t"
    "v_mfma_f32_16x16x32_bf16 v[48:51], v[128:131], v[216:219], v[48:51]\n\t"
    "ds_read_b64_tr_b16 v[184:185], v240 offset:192\n\t"
    "v_mfma_f32_16x16x32_bf16 v[52:55], v[128:131], v[220:223], v[52:55]\n\t"
    "ds_read_b64_tr_b16 v[186:187], v240 offset:1472\n\t"
    "v_mfma_f32_16x16x32_bf16 v[60:63], v[136:139], v[220:223], v[60:63]\n\t"
    "v_mfma_f32_16x16x32_bf16 v[56:59], v[136:139], v[216:219], v[56:59]\n\t"
    "v_mfma_f32_16x16x32_bf16 v[64:67], v[144:147], v[216:219], v[64:67]\n\t"
    "v_mfma_f32_16x16x32_bf16 v[68:71], v[144:147], v[220:223], v[68:71]\n\t"
    "v_mfma_f32_16x16x32_bf16 v[76:79], v[152:155], v[220:223], v[76:79]\n\t"
    "v_mfma_f32_16x16x32_bf16 v[72:75], v[152:155], v[216:219], v[72:75]\n\t"
    "s_branch COM%=\n\t"
    "HL%=:\n\t"
    "s_waitcnt lgkmcnt(0)\n\t"
    "s_barrier\n\t"
    "s_cmp_eq_u32 s40, 2\n\t"
    "s_cbranch_scc1 HB%=\n\t"
    "ds_read_b128 v[216:219], v245 offset:0\n\t"
    "ds_read_b128 v[220:223], v245 offset:1024\n\t"
    "v_mfma_f32_16x16x32_bf16 v[124:127], v[212:215], v[180:183], v[124:127]\n\t"
    "v_cvt_pk_bf16_f32 v112, v112, v113\n\t"
    "v_mfma_f32_16x16x32_bf16 v[120:123], v[208:211], v[180:183], v[120:123]\n\t"
    "v_cvt_pk_bf16_f32 v113, v114, v115\n\t"
    "v_mfma_f32_16x16x32_bf16 v[128:131], v[208:211], v[184:187], v[128:131]\n\t"
    "v_cvt_pk_bf16_f32 v114, v116, v117\n\t"
    "v_mfma_f32_16x16x32_bf16 v[132:135], v[212:215], v[184:187], v[132:135]\n\t"
    "v_cvt_pk_bf16_f32 v115, v118, v119\n\t"
    "v_mfma_f32_16x16x32_bf16 v[140:143], v[212:215], v[188:191], v[140:143]\n\t"
    "v_mfma_f32_16x16x32_bf16 v[136:139], v[208:211], v[188:191], v[136:139]\n\t"
    "s_nop 0\n\t"
    "v_cvt_pk_bf16_f32 v120, v120, v121\n\t"
    "v_mfma_f32_16x16x32_bf16 v[144:147], v[208:211], v[192:195], v[144:147]\n\t"
    "v_cvt_pk_bf16_f32 v121, v122, v123\n\t"
    "v_mfma_f32_16x16x32_bf16 v[148:151], v[212:215], v[192:195], v[148:151]\n\t"
    "v_cvt_pk_bf16_f32 v122, v124, v125\n\t"
    "v_mfma_f32_16x16x32_bf16 v[156:159], v[212:215], v[196:199], v[156:159]\n\t"
    "v_cvt_pk_bf16_f32 v123, v126, v127\n\t"
    "v_mfma_f32_16x16x32_bf16 v[152:155], v[208:211], v[196:199], v[152:155]\n\t"
    "v_cvt_pk_bf16_f32 v128, v128, v129\n\t"
    "s_waitcnt lgkmcnt(0)\n\t"
    "v_mfma_f32_16x16x32_bf16 v[0:3], v[80:83], v[216:219], v[0:3]\n\t"
    "v_cvt_pk_bf16_f32 v129, v130, v131\n\t"
    "ds_read_b128 v[200:203], v242 offset:0\n\t"
    "v_mfma_f32_16x16x32_bf16 v[4:7], v[80:83], v[220:223], v[4:7]\n\t"
    "v_cvt_pk_bf16_f32 v130, v132, v133\n\t"
    "ds_read_b128 v[204:207], v242 offset:2048\n\t"
    "v_mfma_f32_16x16x32_bf16 v[12:15], v[88:91], v[220:223], v[12:15]\n\t"
    "v_cvt_pk_bf16_f32 v131, v134, v135\n\t"
    "ds_read_b64_tr_b16 v[160:161], v240 offset:0\n\t"
    "v_mfma_f32_16x16x32_bf16 v[8:11], v[88:91], v[216:219], v[8:11]\n\t"
    "v_cvt_pk_bf16_f32 v136, v136, v137\n\t"
    "ds_read_b64_tr_b16 v[162:163], v240 offset:1280\n\t"
    "v_mfma_f32_16x16x32_bf16 v[16:19], v[96:99], v[216:219], v[16:19]\n\t"
    "v_cvt_pk_bf16_f32 v137, v138, v139\n\t"
    "ds_read_b64_tr_b16 v[164:165], v241 offset:0\n\t"
    "v_mfma_f32_16x16x32_bf16 v[20:23], v[96:99], v[220:223], v[20:23]\n\t"
    "v_cvt_pk_bf16_f32 v138, v140, v141\n\t"
    "ds_read_b64_tr_b16 v[166:167], v241 offset:1280\n\t"
    "v_mfma_f32_16x16x32_bf16 v[28:31], v[104:107], v[220:223], v[28:31]\n\t"
    "v_cvt_pk_bf16_f32 v139, v142, v143\n\t"
    "ds_read_b64_tr_b16 v[168:169], v240 offset:64\n\t"
    "v_mfma_f32_16x16x32_bf16 v[24:27], v[104:107], v[216:219], v[24:27]\n\t"
    "v_cvt_pk_bf16_f32 v144, v144, v145\n\t"
    "ds_read_b64_tr_b16 v[170:171], v240 offset:1344\n\t"
    "v_mfma_f32_16x16x32_bf16 v[32:35], v[112:115], v[216:219], v[32:35]\n\t"
    "v_cvt_pk_bf16_f32 v145, v146, v147\n\t"
    "ds_read_b64_tr_b16 v[172:173], v241 offset:64\n\t"
    "v_mfma_f32_16x16x32_bf16 v[36:39], v[112:115], v[220:223], v[36:39]\n\t"
    "v_cvt_pk_bf16_f32 v146, v148, v149\n\t"
    "ds_read_b64_tr_b16 v[174:175], v241 offset:1344\n\t"
    "v_mfma_f32_16x16x32_bf16 v[44:47], v[120:123], v[220:223], v[44:47]\n\t"
    "v_cvt_pk_bf16_f32 v147, v150, v151\n\t"
    "ds_read_b64_tr_b16 v[176:177], v240 offset:128\n\t"
    "v_mfma_f32_16x16x32_bf16 v[40:43], v[120:123], v[216:219], v[40:43]\n\t"
    "v_cvt_pk_bf16_f32 v152, v152, v153\n\t"
    "ds_read_b64_tr_b16 v[178:179], v240 offset:1408\n\t"
    "v_mfma_f32_16x16x32_bf16 v[48:51], v[128:131], v[216:219], v[48:51]\n\t"
    "v_cvt_pk_bf16_f32 v153, v154, v155\n\t"
    "ds_read_b64_tr_b16 v[180:181], v241 offset:128\n\t"
    "v_mfma_f32_16x16x32_bf16 v[52:55], v[128:131], v[220:223], v[52:55]\n\t"
    "v_cvt_pk_bf16_f32 v154, v156, v157\n\t"
    "ds_read_b64_tr_b16 v[182:183], v241 offset:1408\n\t"
    "v_mfma_f32_16x16x32_bf16 v[60:63], v[136:139], v[220:223], v[60:63]\n\t"
    "v_cvt_pk_bf16_f32 v155, v158, v159\n\t"
    "ds_read_b64_tr_b16 v[184:185], v240 offset:192\n\t"
    "v_mfma_f32_16x16x32_bf16 v[56:59], v[136:139], v[216:219], v[56:59]\n\t"
    "ds_read_b64_tr_b16 v[186:187], v240 offset:1472\n\t"
    "v_mfma_f32_16x16x32_bf16 v[64:67], v[144:147], v[216:219], v[64:67]\n\t"
    "v_mfma_f32_16x16x32_bf16 v[68:71], v[144:147], v[220:223], v[68:71]\n\t"
    "v_mfma_f32_16x16x32_bf16 v[76:79], v[152:155], v[220:223], v[76:79]\n\t"
    "v_mfma_f32_16x16x32_bf16 v[72:75], v[152:155], v[216:219], v[72:75]\n\t"
    "ds_read_b128 v[216:219], v245 offset:2048\n\t"
    "ds_read_b128 v[220:223], v245 offset:3072\n\t"
    "COM%=:\n\t"
    "s_waitcnt lgkmcnt(7)\n\t"
    "v_mfma_f32_16x16x32_bf16 v[80:83], v[200:203], v[160:163], 0\n\t"
    "ds_read_b64_tr_b16 v[188:189], v241 offset:192\n\t"
    "ds_read_b64_tr_b16 v[190:191], v241 offset:1472\n\t"
    "v_mfma_f32_16x16x32_bf16 v[84:87], v[204:207], v[160:163], 0\n\t"
    "ds_read_b64_tr_b16 v[192:193], v240 offset:256\n\t"
    "s_and_b32 s42, s60, 3\n\t"
    "v_mfma_f32_16x16x32_bf16 v[92:95], v[204:207], v[164:167], 0\n\t"
    "ds_read_b64_tr_b16 v[194:195], v240 offset:1536\n\t"
    "s_mul_i32 s42, s42, s43\n\t"
    "v_mfma_f32_16x16x32_bf16 v[88:91], v[200:203], v[164:167], 0\n\t"
    "ds_read_b64_tr_b16 v[196:197], v241 offset:256\n\t"
    "s_lshr_b32 s60, s60, 2\n\t"
    "v_mfma_f32_16x16x32_bf16 v[96:99], v[200:203], v[168:171], 0\n\t"
    "ds_read_b64_tr_b16 v[198:199], v241 offset:1536\n\t"
    "s_sub_u32 s45, s42, s41\n\t"
    "v_mfma_f32_16x16x32_bf16 v[100:103], v[204:207], v[168:171], 0\n\t"
    "ds_read_b128 v[208:211], v243 offset:0\n\t"
    "s_mov_b32 s41, s42\n\t"
    "s_waitcnt lgkmcnt(7)\n\t"
    "v_mfma_f32_16x16x32_bf16 v[108:111], v[204:207], v[172:175], 0\n\t"
    "ds_read_b128 v[212:215], v243 offset:2048\n\t"
    "v_add_u32 v242, s45, v242\n\t"
    "v_mfma_f32_16x16x32_bf16 v[104:107], v[200:203], v[172:175], 0\n\t"
    "ds_read_b64_tr_b16 v[160:161], v240 offset:10240\n\t"
    "v_add_u32 v243, s45, v243\n\t"
    "v_mfma_f32_16x16x32_bf16 v[112:115], v[200:203], v[176:179], 0\n\t"
    "ds_read_b64_tr_b16 v[162:163], v240 offset:11520\n\t"
    "v_mfma_f32_16x16x32_bf16 v[116:119], v[204:207], v[176:179], 0\n\t"
    "ds_read_b64_tr_b16 v[164:165], v241 offset:10240\n\t"
    "v_mfma_f32_16x16x32_bf16 v[124:127], v[204:207], v[180:183], 0\n\t"
    "ds_read_b64_tr_b16 v[166:167], v241 offset:11520\n\t"
    "v_mfma_f32_16x16x32_bf16 v[120:123], v[200:203], v[180:183], 0\n\t"
    "ds_read_b64_tr_b16 v[168:169], v240 offset:10304\n\t"
    "v_mfma_f32_16x16x32_bf16 v[128:131], v[200:203], v[184:187], 0\n\t"
    "ds_read_b64_tr_b16 v[170:171], v240 offset:11584\n\t"
    "v_mfma_f32_16x16x32_bf16 v[132:135], v[204:207], v[184:187], 0\n\t"
    "ds_read_b64_tr_b16 v[172:173], v241 offset:10304\n\t"
    "s_waitcnt lgkmcnt(7)\n\t"
    "v_mfma_f32_16x16x32_bf16 v[140:143], v[204:207], v[188:191], 0\n\t"
    "ds_read_b64_tr_b16 v[174:175], v241 offset:11584\n\t"
    "v_mfma_f32_16x16x32_bf16 v[136:139], v[200:203], v[188:191], 0\n\t"
    "ds_read_b64_tr_b16 v[176:177], v240 offset:10368\n\t"
    "v_mfma_f32_16x16x32_bf16 v[144:147], v[200:203], v[192:195], 0\n\t"
    "ds_read_b64_tr_b16 v[178:179], v240 offset:11648\n\t"
    "v_mfma_f32_16x16x32_bf16 v[148:151], v[204:207], v[192:195], 0\n\t"
    "ds_read_b64_tr_b16 v[180:181], v241 offset:10368\n\t"
    "v_mfma_f32_16x16x32_bf16 v[156:159], v[204:207], v[196:199], 0\n\t"
    "ds_read_b64_tr_b16 v[182:183], v241 offset:11648\n\t"
    "v_mfma_f32_16x16x32_bf16 v[152:155], v[200:203], v[196:199], 0\n\t"
    "ds_read_b64_tr_b16 v[184:185], v240 offset:10432\n\t"
    "s_waitcnt lgkmcnt(7)\n\t"
    "v_mfma_f32_16x16x32_bf16 v[80:83], v[208:211], v[160:163], v[80:83]\n\t"
    "ds_read_b64_tr_b16 v[186:187], v240 offset:11712\n\t"
    "s_cmp_eq_u32 s40, 3\n\t"
    "v_mfma_f32_16x16x32_bf16 v[84:87], v[212:215], v[160:163], v[84:87]\n\t"
    "ds_read_b64_tr_b16 v[188:189], v241 offset:10432\n\t"
    "v_mfma_f32_16x16x32_bf16 v[92:95], v[212:215], v[164:167], v[92:95]\n\t"
    "ds_read_b64_tr_b16 v[190:191], v241 offset:11712\n\t"
    "v_mfma_f32_16x16x32_bf16 v[88:91], v[208:211], v[164:167], v[88:91]\n\t"
    "ds_read_b64_tr_b16 v[192:193], v240 offset:10496\n\t"
    "v_mfma_f32_16x16x32_bf16 v[96:99], v[208:211], v[168:171], v[96:99]\n\t"
    "ds_read_b64_tr_b16 v[194:195], v240 offset:11776\n\t"
    "v_add_u32 v240, s45, v240\n\t"
    "v_mfma_f32_16x16x32_bf16 v[100:103], v[212:215], v[168:171], v[100:103]\n\t"
    "ds_read_b64_tr_b16 v[196:197], v241 offset:10496\n\t"
    "s_waitcnt lgkmcnt(7)\n\t"
    "v_mfma_f32_16x16x32_bf16 v[108:111], v[212:215], v[172:175], v[108:111]\n\t"
    "ds_read_b64_tr_b16 v[198:199], v241 offset:11776\n\t"
    "v_add_u32 v241, s45, v241\n\t"
    "v_mfma_f32_16x16x32_bf16 v[104:107], v[208:211], v[172:175], v[104:107]\n\t"
    "v_mfma_f32_16x16x32_bf16 v[112:115], v[208:211], v[176:179], v[112:115]\n\t"
    "v_mfma_f32_16x16x32_bf16 v[116:119], v[212:215], v[176:179], v[116:119]\n\t"
    "s_waitcnt lgkmcnt(0)\n\t"
    "s_barrier\n\t"
    "s_cbranch_scc1 LAST%=\n\t"
    "v_mfma_f32_16x16x32_bf16 v[124:127], v[212:215], v[180:183], v[124:127]\n\t"
    "ds_read_b128 v[200:203], v242 offset:0\n\t"
    "ds_read_b128 v[204:207], v242 offset:2048\n\t"
    "v_mfma_f32_16x16x32_bf16 v[120:123], v[208:211], v[180:183], v[120:123]\n\t"
    "ds_read_b64_tr_b16 v[160:161], v240 offset:0\n\t"
    "ds_read_b64_tr_b16 v[162:163], v240 offset:1280\n\t"
    "v_mfma_f32_16x16x32_bf16 v[128:131], v[208:211], v[184:187], v[128:131]\n\t"
    "ds_read_b64_tr_b16 v[164:165], v241 offset:0\n\t"
    "ds_read_b64_tr_b16 v[166:167], v241 offset:1280\n\t"
    "v_mfma_f32_16x16x32_bf16 v[132:135], v[212:215], v[184:187], v[132:135]\n\t"
    "ds_read_b64_tr_b16 v[168:169], v240 offset:64\n\t"
    "ds_read_b64_tr_b16 v[170:171], v240 offset:1344\n\t"
    "v_mfma_f32_16x16x32_bf16 v[140:143], v[212:215], v[188:191], v[140:143]\n\t"
    "ds_read_b64_tr_b16 v[172:173], v241 offset:64\n\t"
    "ds_read_b64_tr_b16 v[174:175], v241 offset:1344\n\t"
    "v_mfma_f32_16x16x32_bf16 v[136:139], v[208:211], v[188:191], v[136:139]\n\t"
    "ds_read_b64_tr_b16 v[176:177], v240 offset:128\n\t"
    "ds_read_b64_tr_b16 v[178:179], v240 offset:1408\n\t"
    "v_mfma_f32_16x16x32_bf16 v[144:147], v[208:211], v[192:195], v[144:147]\n\t"
    "ds_read_b64_tr_b16 v[180:181], v241 offset:128\n\t"
    "ds_read_b64_tr_b16 v[182:183], v241 offset:1408\n\t"
    "v_mfma_f32_16x16x32_bf16 v[148:151], v[212:215], v[192:195], v[148:151]\n\t"
    "ds_read_b64_tr_b16 v[184:185], v240 offset:192\n\t"
    "s_and_b32 s42, s60, 3\n\t"
    "v_mfma_f32_16x16x32_bf16 v[156:159], v[212:215], v[196:199], v[156:159]\n\t"
    "ds_read_b64_tr_b16 v[186:187], v240 offset:1472\n\t"
    "s_mul_i32 s42, s42, s43\n\t"
    "v_mfma_f32_16x16x32_bf16 v[152:155], v[208:211], v[196:199], v[152:155]\n\t"
    "ds_read_b64_tr_b16 v[188:189], v241 offset:192\n\t"
    "s_lshr_b32 s60, s60, 2\n\t"
    "s_waitcnt lgkmcnt(7)\n\t"
    "v_mfma_f32_16x16x32_bf16 v[80:83], v[200:203], v[160:163], v[80:83]\n\t"
    "ds_read_b64_tr_b16 v[190:191], v241 offset:1472\n\t"
    "s_sub_u32 s45, s42, s41\n\t"
    "v_mfma_f32_16x16x32_bf16 v[84:87], v[204:207], v[160:163], v[84:87]\n\t"
    "ds_read_b64_tr_b16 v[192:193], v240 offset:256\n\t"
    "s_mov_b32 s41, s42\n\t"
    "v_mfma_f32_16x16x32_bf16 v[92:95], v[204:207], v[164:167], v[92:95]\n\t"
    "ds_read_b64_tr_b16 v[194:195], v240 offset:1536\n\t"
    "v_add_u32 v242, s45, v242\n\t"
    "v_mfma_f32_16x16x32_bf16 v[88:91], v[200:203], v[164:167], v[88:91]\n\t"
    "ds_read_b64_tr_b16 v[196:197], v241 offset:256\n\t"
    "v_mfma_f32_16x16x32_bf16 v[96:99], v[200:203], v[168:171], v[96:99]\n\t"
    "ds_read_b64_tr_b16 v[198:199], v241 offset:1536\n\t"
    "v_mfma_f32_16x16x32_bf16 v[100:103], v[204:207], v[168:171], v[100:103]\n\t"
    "ds_read_b128 v[208:211], v243 offset:0\n\t"
    "v_mfma_f32_16x16x32_bf16 v[108:111], v[204:207], v[172:175], v[108:111]\n\t"
    "ds_read_b128 v[212:215], v243 offset:2048\n\t"
    "v_add_u32 v243, s45, v243\n\t"
    "v_mfma_f32_16x16x32_bf16 v[104:107], v[200:203], v[172:175], v[104:107]\n\t"
    "ds_read_b64_tr_b16 v[160:161], v240 offset:10240\n\t"
    "s_waitcnt lgkmcnt(7)\n\t"
    "v_mfma_f32_16x16x32_bf16 v[112:115], v[200:203], v[176:179], v[112:115]\n\t"
    "ds_read_b64_tr_b16 v[162:163], v240 offset:11520\n\t"
    "v_mfma_f32_16x16x32_bf16 v[116:119], v[204:207], v[176:179], v[116:119]\n\t"
    "ds_read_b64_tr_b16 v[164:165], v241 offset:10240\n\t"
    "v_mfma_f32_16x16x32_bf16 v[124:127], v[204:207], v[180:183], v[124:127]\n\t"
    "ds_read_b64_tr_b16 v[166:167], v241 offset:11520\n\t"
    "v_mfma_f32_16x16x32_bf16 v[120:123], v[200:203], v[180:183], v[120:123]\n\t"
    "ds_read_b64_tr_b16 v[168:169], v240 offset:10304\n\t"
    "v_mfma_f32_16x16x32_bf16 v[128:131], v[200:203], v[184:187], v[128:131]\n\t"
    "ds_read_b64_tr_b16 v[170:171], v240 offset:11584\n\t"
    "v_mfma_f32_16x16x32_bf16 v[132:135], v[204:207], v[184:187], v[132:135]\n\t"
    "ds_read_b64_tr_b16 v[172:173], v241 offset:10304\n\t"
    "v_mfma_f32_16x16x32_bf16 v[140:143], v[204:207], v[188:191], v[140:143]\n\t"
    "ds_read_b64_tr_b16 v[174:175], v241 offset:11584\n\t"
    "v_mfma_f32_16x16x32_bf16 v[136:139], v[200:203], v[188:191], v[136:139]\n\t"
    "ds_read_b64_tr_b16 v[176:177], v240 offset:10368\n\t"
    "s_waitcnt lgkmcnt(7)\n\t"
    "v_mfma_f32_16x16x32_bf16 v[144:147], v[200:203], v[192:195], v[144:147]\n\t"
    "ds_read_b64_tr_b16 v[178:179], v240 offset:11648\n\t"
    "v_mfma_f32_16x16x32_bf16 v[148:151], v[204:207], v[192:195], v[148:151]\n\t"
    "ds_read_b64_tr_b16 v[180:181], v241 offset:10368\n\t"
    "v_mfma_f32_16x16x32_bf16 v[156:159], v[204:207], v[196:199], v[156:159]\n\t"
    "ds_read_b64_tr_b16 v[182:183], v241 offset:11648\n\t"
    "v_mfma_f32_16x16x32_bf16 v[152:155], v[200:203], v[196:199], v[152:155]\n\t"
    "ds_read_b64_tr_b16 v[184:185], v240 offset:10432\n\t"
    "v_mfma_f32_16x16x32_bf16 v[80:83], v[208:211], v[160:163], v[80:83]\n\t"
    "ds_read_b64_tr_b16 v[186:187], v240 offset:11712\n\t"
    "v_mfma_f32_16x16x32_bf16 v[84:87], v[212:215], v[160:163], v[84:87]\n\t"
    "ds_read_b64_tr_b16 v[188:189], v241 offset:10432\n\t"
    "s_waitcnt lgkmcnt(7)\n\t"
    "v_mfma_f32_16x16x32_bf16 v[92:95], v[212:215], v[164:167], v[92:95]\n\t"
    "ds_read_b64_tr_b16 v[190:191], v241 offset:11712\n\t"
    "v_mfma_f32_16x16x32_bf16 v[88:91], v[208:211], v[164:167], v[88:91]\n\t"
    "ds_read_b64_tr_b16 v[192:193], v240 offset:10496\n\t"
    "v_mfma_f32_16x16x32_bf16 v[96:99], v[208:211], v[168:171], v[96:99]\n\t"
    "ds_read_b64_tr_b16 v[194:195], v240 offset:11776\n\t"
    "v_add_u32 v240, s45, v240\n\t"
    "v_mfma_f32_16x16x32_bf16 v[100:103], v[212:215], v[168:171], v[100:103]\n\t"
    "ds_read_b64_tr_b16 v[196:197], v241 offset:10496\n\t"
    "v_mfma_f32_16x16x32_bf16 v[108:111], v[212:215], v[172:175], v[108:111]\n\t"
    "ds_read_b64_tr_b16 v[198:199], v241 offset:11776\n\t"
    "v_add_u32 v241, s45, v241\n\t"
    "v_mfma_f32_16x16x32_bf16 v[104:107], v[208:211], v[172:175], v[104:107]\n\t"
    "s_waitcnt lgkmcnt(7)\n\t"
    "v_mfma_f32_16x16x32_bf16 v[112:115], v[208:211], v[176:179], v[112:115]\n\t"
    "v_mfma_f32_16x16x32_bf16 v[116:119], v[212:215], v[176:179], v[116:119]\n\t"
    "s_waitcnt lgkmcnt(0)\n\t"
    "s_barrier\n\t"
    "v_mfma_f32_16x16x32_bf16 v[124:127], v[212:215], v[180:183], v[124:127]\n\t"
    "ds_read_b128 v[200:203], v242 offset:0\n\t"
    "ds_read_b128 v[204:207], v242 offset:2048\n\t"
    "v_mfma_f32_16x16x32_bf16 v[120:123], v[208:211], v[180:183], v[120:123]\n\t"
    "ds_read_b64_tr_b16 v[160:161], v240 offset:0\n\t"
    "ds_read_b64_tr_b16 v[162:163], v240 offset:1280\n\t"
    "v_mfma_f32_16x16x32_bf16 v[128:131], v[208:211], v[184:187], v[128:131]\n\t"
    "ds_read_b64_tr_b16 v[164:165], v241 offset:0\n\t"
    "ds_read_b64_tr_b16 v[166:167], v241 offset:1280\n\t"
    "v_mfma_f32_16x16x32_bf16 v[132:135], v[212:215], v[184:187], v[132:135]\n\t"
    "ds_read_b64_tr_b16 v[168:169], v240 offset:64\n\t"
    "ds_read_b64_tr_b16 v[170:171], v240 offset:1344\n\t"
    "v_mfma_f32_16x16x32_bf16 v[140:143], v[212:215], v[188:191], v[140:143]\n\t"
    "ds_read_b64_tr_b16 v[172:173], v241 offset:64\n\t"
    "ds_read_b64_tr_b16 v[174:175], v241 offset:1344\n\t"
    "v_mfma_f32_16x16x32_bf16 v[136:139], v[208:211], v[188:191], v[136:139]\n\t"
    "ds_read_b64_tr_b16 v[176:177], v240 offset:128\n\t"
    "ds_read_b64_tr_b16 v[178:179], v240 offset:1408\n\t"
    "v_mfma_f32_16x16x32_bf16 v[144:147], v[208:211], v[192:195], v[144:147]\n\t"
    "ds_read_b64_tr_b16 v[180:181], v240 offset:10240\n\t"
    "ds_read_b64_tr_b16 v[182:183], v240 offset:11520\n\t"
    "v_mfma_f32_16x16x32_bf16 v[148:151], v[212:215], v[192:195], v[148:151]\n\t"
    "ds_read_b64_tr_b16 v[184:185], v241 offset:10240\n\t"
    "s_and_b32 s42, s60, 3\n\t"
    "v_mfma_f32_16x16x32_bf16 v[156:159], v[212:215], v[196:199], v[156:159]\n\t"
    "ds_read_b64_tr_b16 v[186:187], v241 offset:11520\n\t"
    "s_mul_i32 s42, s42, s43\n\t"
    "v_mfma_f32_16x16x32_bf16 v[152:155], v[208:211], v[196:199], v[152:155]\n\t"
    "ds_read_b64_tr_b16 v[188:189], v240 offset:10304\n\t"
    "s_lshr_b32 s60, s60, 2\n\t"
    "s_waitcnt lgkmcnt(7)\n\t"
    "v_mfma_f32_16x16x32_bf16 v[80:83], v[200:203], v[160:163], v[80:83]\n\t"
    "ds_read_b64_tr_b16 v[190:191], v240 offset:11584\n\t"
    "s_sub_u32 s45, s42, s41\n\t"
    "v_mfma_f32_16x16x32_bf16 v[84:87], v[204:207], v[160:163], v[84:87]\n\t"
    "ds_read_b64_tr_b16 v[192:193], v241 offset:10304\n\t"
    "s_mov_b32 s41, s42\n\t"
    "v_mfma_f32_16x16x32_bf16 v[92:95], v[204:207], v[164:167], v[92:95]\n\t"
    "ds_read_b64_tr_b16 v[194:195], v241 offset:11584\n\t"
    "v_add_u32 v242, s45, v242\n\t"
    "v_mfma_f32_16x16x32_bf16 v[88:91], v[200:203], v[164:167], v[88:91]\n\t"
    "ds_read_b128 v[208:211], v243 offset:0\n\t"
    "s_add_u32 s40, s40, 1\n\t"
    "v_mfma_f32_16x16x32_bf16 v[96:99], v[200:203], v[168:171], v[96:99]\n\t"
    "ds_read_b128 v[212:215], v243 offset:2048\n\t"
    "v_add_u32 v243, s45, v243\n\t"
    "v_mfma_f32_16x16x32_bf16 v[100:103], v[204:207], v[168:171], v[100:103]\n\t"
    "ds_read_b64_tr_b16 v[196:197], v240 offset:10368\n\t"
    "v_mfma_f32_16x16x32_bf16 v[108:111], v[204:207], v[172:175], v[108:111]\n\t"
    "ds_read_b64_tr_b16 v[198:199], v240 offset:11648\n\t"
    "v_mfma_f32_16x16x32_bf16 v[104:107], v[200:203], v[172:175], v[104:107]\n\t"
    "ds_read_b64_tr_b16 v[160:161], v241 offset:128\n\t"
    "s_waitcnt lgkmcnt(7)\n\t"
    "v_mfma_f32_16x16x32_bf16 v[112:115], v[200:203], v[176:179], v[112:115]\n\t"
    "ds_read_b64_tr_b16 v[162:163], v241 offset:1408\n\t"
    "v_mfma_f32_16x16x32_bf16 v[116:119], v[204:207], v[176:179], v[116:119]\n\t"
    "ds_read_b64_tr_b16 v[164:165], v240 offset:192\n\t"
    "s_waitcnt lgkmcnt(5)\n\t"
    "v_mfma_f32_16x16x32_bf16 v[84:87], v[212:215], v[180:183], v[84:87]\n\t"
    "ds_read_b64_tr_b16 v[166:167], v240 offset:1472\n\t"
    "v_mfma_f32_16x16x32_bf16 v[80:83], v[208:211], v[180:183], v[80:83]\n\t"
    "ds_read_b64_tr_b16 v[168:169], v241 offset:192\n\t"
    "v_mfma_f32_16x16x32_bf16 v[88:91], v[208:211], v[184:187], v[88:91]\n\t"
    "ds_read_b64_tr_b16 v[170:171], v241 offset:1472\n\t"
    "v_mfma_f32_16x16x32_bf16 v[92:95], v[212:215], v[184:187], v[92:95]\n\t"
    "ds_read_b64_tr_b16 v[172:173], v240 offset:256\n\t"
    "v_mfma_f32_16x16x32_bf16 v[100:103], v[212:215], v[188:191], v[100:103]\n\t"
    "ds_read_b64_tr_b16 v[174:175], v240 offset:1536\n\t"
    "v_mfma_f32_16x16x32_bf16 v[96:99], v[208:211], v[188:191], v[96:99]\n\t"
    "ds_read_b64_tr_b16 v[176:177], v241 offset:256\n\t"
    "v_cvt_pk_bf16_f32 v80, v80, v81\n\t"
    "v_mfma_f32_16x16x32_bf16 v[104:107], v[208:211], v[192:195], v[104:107]\n\t"
    "ds_read_b64_tr_b16 v[178:179], v241 offset:1536\n\t"
    "v_cvt_pk_bf16_f32 v81, v82, v83\n\t"
    "v_mfma_f32_16x16x32_bf16 v[108:111], v[212:215], v[192:195], v[108:111]\n\t"
    "ds_read_b64_tr_b16 v[180:181], v241 offset:10368\n\t"
    "v_cvt_pk_bf16_f32 v82, v84, v85\n\t"
    "s_waitcnt lgkmcnt(7)\n\t"
    "v_mfma_f32_16x16x32_bf16 v[116:119], v[212:215], v[196:199], v[116:119]\n\t"
    "ds_read_b64_tr_b16 v[182:183], v241 offset:11648\n\t"
    "v_cvt_pk_bf16_f32 v83, v86, v87\n\t"
    "v_mfma_f32_16x16x32_bf16 v[112:115], v[208:211], v[196:199], v[112:115]\n\t"
    "ds_read_b64_tr_b16 v[184:185], v240 offset:10432\n\t"
    "v_cvt_pk_bf16_f32 v88, v88, v89\n\t"
    "v_mfma_f32_16x16x32_bf16 v[120:123], v[200:203], v[160:163], v[120:123]\n\t"
    "ds_read_b64_tr_b16 v[186:187], v240 offset:11712\n\t"
    "v_cvt_pk_bf16_f32 v89, v90, v91\n\t"
    "v_mfma_f32_16x16x32_bf16 v[124:127], v[204:207], v[160:163], v[124:127]\n\t"
    "ds_read_b64_tr_b16 v[188:189], v241 offset:10432\n\t"
    "v_cvt_pk_bf16_f32 v90, v92, v93\n\t"
    "v_mfma_f32_16x16x32_bf16 v[132:135], v[204:207], v[164:167], v[132:135]\n\t"
    "ds_read_b64_tr_b16 v[190:191], v241 offset:11712\n\t"
    "v_cvt_pk_bf16_f32 v91, v94, v95\n\t"
    "v_mfma_f32_16x16x32_bf16 v[128:131], v[200:203], v[164:167], v[128:131]\n\t"
    "ds_read_b64_tr_b16 v[192:193], v240 offset:10496\n\t"
    "v_cvt_pk_bf16_f32 v96, v96, v97\n\t"
    "s_waitcnt lgkmcnt(7)\n\t"
    "v_mfma_f32_16x16x32_bf16 v[136:139], v[200:203], v[168:171], v[136:139]\n\t"
    "ds_read_b64_tr_b16 v[194:195], v240 offset:11776\n\t"
    "v_add_u32 v240, s45, v240\n\t"
    "v_mfma_f32_16x16x32_bf16 v[140:143], v[204:207], v[168:171], v[140:143]\n\t"
    "ds_read_b64_tr_b16 v[196:197], v241 offset:10496\n\t"
    "v_cvt_pk_bf16_f32 v97, v98, v99\n\t"
    "v_mfma_f32_16x16x32_bf16 v[148:151], v[204:207], v[172:175], v[148:151]\n\t"
    "ds_read_b64_tr_b16 v[198:199], v241 offset:11776\n\t"
    "v_add_u32 v241, s45, v241\n\t"
    "v_mfma_f32_16x16x32_bf16 v[144:147], v[200:203], v[172:175], v[144:147]\n\t"
    "v_cvt_pk_bf16_f32 v98, v100, v101\n\t"
    "v_cvt_pk_bf16_f32 v99, v102, v103\n\t"
    "v_mfma_f32_16x16x32_bf16 v[152:155], v[200:203], v[176:179], v[152:155]\n\t"
    "v_cvt_pk_bf16_f32 v104, v104, v105\n\t"
    "v_cvt_pk_bf16_f32 v105, v106, v107\n\t"
    "v_mfma_f32_16x16x32_bf16 v[156:159], v[204:207], v[176:179], v[156:159]\n\t"
    "v_cvt_pk_bf16_f32 v106, v108, v109\n\t"
    "v_cvt_pk_bf16_f32 v107, v110, v111\n\t"
    "s_branch HL%=\n\t"
    "LAST%=:\n\t"
    "s_and_b32 s42, s60, 3\n\t"
    "s_mul_i32 s42, s42, s43\n\t"
    "s_sub_u32 s45, s42, s41\n\t"
    "v_mfma_f32_16x16x32_bf16 v[124:127], v[212:215], v[180:183], v[124:127]\n\t"
    "ds_read_b128 v[200:203], v242 offset:0\n\t"
    "ds_read_b128 v[204:207], v242 offset:2048\n\t"
    "v_add_u32 v234, s45, v240\n\t"
    "v_mfma_f32_16x16x32_bf16 v[120:123], v[208:211], v[180:183], v[120:123]\n\t"
    "ds_read_b64_tr_b16 v[160:161], v240 offset:0\n\t"
    "ds_read_b64_tr_b16 v[162:163], v240 offset:1280\n\t"
    "v_add_u32 v235, s45, v241\n\t"
    "v_mfma_f32_16x16x32_bf16 v[128:131], v[208:211], v[184:187], v[128:131]\n\t"
    "ds_read_b64_tr_b16 v[164:165], v241 offset:0\n\t"
    "ds_read_b64_tr_b16 v[166:167], v241 offset:1280\n\t"
    "v_add_u32 v236, s45, v242\n\t"
    "v_mfma_f32_16x16x32_bf16 v[132:135], v[212:215], v[184:187], v[132:135]\n\t"
    "ds_read_b128 v[224:227], v243 offset:0\n\t"
    "ds_read_b128 v[228:231], v243 offset:2048\n\t"
    "v_add_u32 v237, s45, v243\n\t"
    "v_mfma_f32_16x16x32_bf16 v[140:143], v[212:215], v[188:191], v[140:143]\n\t"
    "ds_read_b64_tr_b16 v[168:169], v240 offset:10240\n\t"
    "ds_read_b64_tr_b16 v[170:171], v240 offset:11520\n\t"
    "s_add_u32 s42, s66, 0x1000\n\t"
    "v_mfma_f32_16x16x32_bf16 v[136:139], v[208:211], v[188:191], v[136:139]\n\t"
    "ds_read_b64_tr_b16 v[172:173], v241 offset:10240\n\t"
    "ds_read_b64_tr_b16 v[174:175], v241 offset:11520\n\t"
    "s_mov_b32 s44, s70\n\t"
    "v_mfma_f32_16x16x32_bf16 v[144:147], v[208:211], v[192:195], v[144:147]\n\t"
    "ds_read_b64_tr_b16 v[176:177], v234 offset:0\n\t"
    "ds_read_b64_tr_b16 v[178:179], v234 offset:1280\n\t"
    "s_add_u32 s45, s44, 0x1000\n\t"
    "v_mfma_f32_16x16x32_bf16 v[148:151], v[212:215], v[192:195], v[148:151]\n\t"
    "ds_read_b64_tr_b16 v[180:181], v235 offset:0\n\t"
    "ds_read_b64_tr_b16 v[182:183], v235 offset:1280\n\t"
    "v_mfma_f32_16x16x32_bf16 v[156:159], v[212:215], v[196:199], v[156:159]\n\t"
    "ds_read_b64_tr_b16 v[184:185], v234 offset:10240\n\t"
    "ds_read_b64_tr_b16 v[186:187], v234 offset:11520\n\t"
    "v_mfma_f32_16x16x32_bf16 v[152:155], v[208:211], v[196:199], v[152:155]\n\t"
    "ds_read_b128 v[208:211], v236 offset:0\n\t"
    "ds_read_b128 v[212:215], v236 offset:2048\n\t"
    "s_waitcnt lgkmcnt(7)\n\t"
    "v_mfma_f32_16x16x32_bf16 v[80:83], v[200:203], v[160:163], v[80:83]\n\t"
    "ds_read_b64_tr_b16 v[188:189], v235 offset:10240\n\t"
    "ds_read_b64_tr_b16 v[190:191], v235 offset:11520\n\t"
    "v_mfma_f32_16x16x32_bf16 v[84:87], v[204:207], v[160:163], v[84:87]\n\t"
    "ds_read_b128 v[192:195], v237 offset:0\n\t"
    "ds_read_b128 v[196:199], v237 offset:2048\n\t"
    "v_mfma_f32_16x16x32_bf16 v[92:95], v[204:207], v[164:167], v[92:95]\n\t"
    "ds_read_b64_tr_b16 v[160:161], v240 offset:64\n\t"
    "ds_read_b64_tr_b16 v[162:163], v240 offset:1344\n\t"
    "v_mfma_f32_16x16x32_bf16 v[88:91], v[200:203], v[164:167], v[88:91]\n\t"
    "ds_read_b64_tr_b16 v[164:165], v241 offset:64\n\t"
    "v_mfma_f32_16x16x32_bf16 v[80:83], v[224:227], v[168:171], v[80:83]\n\t"
    "ds_read_b64_tr_b16 v[166:167], v241 offset:1344\n\t"
    "v_mfma_f32_16x16x32_bf16 v[84:87], v[228:231], v[168:171], v[84:87]\n\t"
    "ds_read_b64_tr_b16 v[168:169], v240 offset:10304\n\t"
    "v_mfma_f32_16x16x32_bf16 v[92:95], v[228:231], v[172:175], v[92:95]\n\t"
    "ds_read_b64_tr_b16 v[170:171], v240 offset:11584\n\t"
    "v_mfma_f32_16x16x32_bf16 v[88:91], v[224:227], v[172:175], v[88:91]\n\t"
    "ds_read_b64_tr_b16 v[172:173], v241 offset:10304\n\t"
    "s_waitcnt lgkmcnt(7)\n\t"
    "v_mfma_f32_16x16x32_bf16 v[80:83], v[208:211], v[176:179], v[80:83]\n\t"
    "ds_read_b64_tr_b16 v[174:175], v241 offset:11584\n\t"
    "v_mfma_f32_16x16x32_bf16 v[84:87], v[212:215], v[176:179], v[84:87]\n\t"
    "ds_read_b64_tr_b16 v[176:177], v234 offset:64\n\t"
    "v_mfma_f32_16x16x32_bf16 v[92:95], v[212:215], v[180:183], v[92:95]\n\t"
    "ds_read_b64_tr_b16 v[178:179], v234 offset:1344\n\t"
    "v_mfma_f32_16x16x32_bf16 v[88:91], v[208:211], v[180:183], v[88:91]\n\t"
    "ds_read_b64_tr_b16 v[180:181], v235 offset:64\n\t"
    "v_mfma_f32_16x16x32_bf16 v[80:83], v[192:195], v[184:187], v[80:83]\n\t"
    "ds_read_b64_tr_b16 v[182:183], v235 offset:1344\n\t"
    "v_mfma_f32_16x16x32_bf16 v[84:87], v[196:199], v[184:187], v[84:87]\n\t"
    "ds_read_b64_tr_b16 v[184:185], v234 offset:10304\n\t"
    "v_mfma_f32_16x16x32_bf16 v[92:95], v[196:199], v[188:191], v[92:95]\n\t"
    "ds_read_b64_tr_b16 v[186:187], v234 offset:11584\n\t"
    "v_mfma_f32_16x16x32_bf16 v[88:91], v[192:195], v[188:191], v[88:91]\n\t"
    "ds_read_b64_tr_b16 v[188:189], v235 offset:10304\n\t"
    "s_waitcnt lgkmcnt(7)\n\t"
    "v_mfma_f32_16x16x32_bf16 v[96:99], v[200:203], v[160:163], v[96:99]\n\t"
    "ds_read_b64_tr_b16 v[190:191], v235 offset:11584\n\t"
    "v_cvt_pk_bf16_f32 v80, v80, v81\n\t"
    "v_cvt_pk_bf16_f32 v81, v82, v83\n\t"
    "v_mfma_f32_16x16x32_bf16 v[100:103], v[204:207], v[160:163], v[100:103]\n\t"
    "ds_read_b64_tr_b16 v[160:161], v240 offset:128\n\t"
    "v_cvt_pk_bf16_f32 v82, v84, v85\n\t"
    "v_cvt_pk_bf16_f32 v83, v86, v87\n\t"
    "v_mfma_f32_16x16x32_bf16 v[108:111], v[204:207], v[164:167], v[108:111]\n\t"
    "ds_read_b64_tr_b16 v[162:163], v240 offset:1408\n\t"
    "v_cvt_pk_bf16_f32 v88, v88, v89\n\t"
    "v_cvt_pk_bf16_f32 v89, v90, v91\n\t"
    "v_mfma_f32_16x16x32_bf16 v[104:107], v[200:203], v[164:167], v[104:107]\n\t"
    "ds_read_b64_tr_b16 v[164:165], v241 offset:128\n\t"
    "v_cvt_pk_bf16_f32 v90, v92, v93\n\t"
    "v_cvt_pk_bf16_f32 v91, v94, v95\n\t"
    "v_mfma_f32_16x16x32_bf16 v[96:99], v[224:227], v[168:171], v[96:99]\n\t"
    "ds_read_b64_tr_b16 v[166:167], v241 offset:1408\n\t"
    "v_mfma_f32_16x16x32_bf16 v[100:103], v[228:231], v[168:171], v[100:103]\n\t"
    "ds_read_b64_tr_b16 v[168:169], v240 offset:10368\n\t"
    "v_mfma_f32_16x16x32_bf16 v[0:3], v[80:83], v[216:219], v[0:3]\n\t"
    "ds_read_b64_tr_b16 v[170:171], v240 offset:11648\n\t"
    "v_mfma_f32_16x16x32_bf16 v[4:7], v[80:83], v[220:223], v[4:7]\n\t"
    "v_mfma_f32_16x16x32_bf16 v[12:15], v[88:91], v[220:223], v[12:15]\n\t"
    "v_mfma_f32_16x16x32_bf16 v[8:11], v[88:91], v[216:219], v[8:11]\n\t"
    "v_mfma_f32_16x16x32_bf16 v[108:111], v[228:231], v[172:175], v[108:111]\n\t"
    "v_mfma_f32_16x16x32_bf16 v[104:107], v[224:227], v[172:175], v[104:107]\n\t"
    "ds_read_b64_tr_b16 v[172:173], v241 offset:10368\n\t"
    "s_nop 0\n\t"
    "v_cvt_pk_bf16_f32 v0, v0, v1\n\t"
    "v_cvt_pk_bf16_f32 v1, v2, v3\n\t"
    "s_waitcnt lgkmcnt(7)\n\t"
    "v_mfma_f32_16x16x32_bf16 v[96:99], v[208:211], v[176:179], v[96:99]\n\t"
    "ds_read_b64_tr_b16 v[174:175], v241 offset:11648\n\t"
    "v_cvt_pk_bf16_f32 v2, v8, v9\n\t"
    "v_cvt_pk_bf16_f32 v3, v10, v11\n\t"
    "v_mfma_f32_16x16x32_bf16 v[100:103], v[212:215], v[176:179], v[100:103]\n\t"
    "ds_read_b64_tr_b16 v[176:177], v234 offset:128\n\t"
    "buffer_store_dwordx4 v[0:3], v244, s[72:75], s66 offen offset:0 sc1\n\t"
    "v_cvt_pk_bf16_f32 v4, v4, v5\n\t"
    "v_mfma_f32_16x16x32_bf16 v[108:111], v[212:215], v[180:183], v[108:111]\n\t"
    "ds_read_b64_tr_b16 v[178:179], v234 offset:1408\n\t"
    "v_cvt_pk_bf16_f32 v5, v6, v7\n\t"
    "v_cvt_pk_bf16_f32 v6, v12, v13\n\t"
    "v_mfma_f32_16x16x32_bf16 v[104:107], v[208:211], v[180:183], v[104:107]\n\t"
    "ds_read_b64_tr_b16 v[180:181], v235 offset:128\n\t"
    "v_cvt_pk_bf16_f32 v7, v14, v15\n\t"
    "buffer_store_dwordx4 v[4:7], v244, s[76:79], s44 offen offset:0 sc1\n\t"
    "v_mfma_f32_16x16x32_bf16 v[96:99], v[192:195], v[184:187], v[96:99]\n\t"
    "ds_read_b64_tr_b16 v[182:183], v235 offset:1408\n\t"
    "v_mfma_f32_16x16x32_bf16 v[100:103], v[196:199], v[184:187], v[100:103]\n\t"
    "ds_read_b64_tr_b16 v[184:185], v234 offset:10368\n\t"
    "v_mfma_f32_16x16x32_bf16 v[108:111], v[196:199], v[188:191], v[108:111]\n\t"
    "ds_read_b64_tr_b16 v[186:187], v234 offset:11648\n\t"
    "v_mfma_f32_16x16x32_bf16 v[104:107], v[192:195], v[188:191], v[104:107]\n\t"
    "ds_read_b64_tr_b16 v[188:189], v235 offset:10368\n\t"
    "s_waitcnt lgkmcnt(7)\n\t"
    "v_mfma_f32_16x16x32_bf16 v[112:115], v[200:203], v[160:163], v[112:115]\n\t"
    "ds_read_b64_tr_b16 v[190:191], v235 offset:11648\n\t"
    "v_cvt_pk_bf16_f32 v96, v96, v97\n\t"
    "v_cvt_pk_bf16_f32 v97, v98, v99\n\t"
    "v_mfma_f32_16x16x32_bf16 v[116:119], v[204:207], v[160:163], v[116:119]\n\t"
    "ds_read_b64_tr_b16 v[160:161], v240 offset:192\n\t"
    "v_cvt_pk_bf16_f32 v98, v100, v101\n\t"
    "v_cvt_pk_bf16_f32 v99, v102, v103\n\t"
    "v_mfma_f32_16x16x32_bf16 v[124:127], v[204:207], v[164:167], v[124:127]\n\t"
    "ds_read_b64_tr_b16 v[162:163], v240 offset:1472\n\t"
    "v_cvt_pk_bf16_f32 v104, v104, v105\n\t"
    "v_cvt_pk_bf16_f32 v105, v106, v107\n\t"
    "v_mfma_f32_16x16x32_bf16 v[120:123], v[200:203], v[164:167], v[120:123]\n\t"
    "ds_read_b64_tr_b16 v[164:165], v241 offset:192\n\t"
    "v_cvt_pk_bf16_f32 v106, v108, v109\n\t"
    "v_cvt_pk_bf16_f32 v107, v110, v111\n\t"
    "v_mfma_f32_16x16x32_bf16 v[112:115], v[224:227], v[168:171], v[112:115]\n\t"
    "ds_read_b64_tr_b16 v[166:167], v241 offset:1472\n\t"
    "v_mfma_f32_16x16x32_bf16 v[116:119], v[228:231], v[168:171], v[116:119]\n\t"
    "ds_read_b64_tr_b16 v[168:169], v240 offset:10432\n\t"
    "v_mfma_f32_16x16x32_bf16 v[16:19], v[96:99], v[216:219], v[16:19]\n\t"
    "ds_read_b64_tr_b16 v[170:171], v240 offset:11712\n\t"
    "v_mfma_f32_16x16x32_bf16 v[20:23], v[96:99], v[220:223], v[20:23]\n\t"
    "v_mfma_f32_16x16x32_bf16 v[28:31], v[104:107], v[220:223], v[28:31]\n\t"
    "v_mfma_f32_16x16x32_bf16 v[24:27], v[104:107], v[216:219], v[24:27]\n\t"
    "v_mfma_f32_16x16x32_bf16 v[124:127], v[228:231], v[172:175], v[124:127]\n\t"
    "v_mfma_f32_16x16x32_bf16 v[120:123], v[224:227], v[172:175], v[120:123]\n\t"
    "ds_read_b64_tr_b16 v[172:173], v241 offset:10432\n\t"
    "s_nop 0\n\t"
    "v_cvt_pk_bf16_f32 v16, v16, v17\n\t"
    "v_cvt_pk_bf16_f32 v17, v18, v19\n\t"
    "s_waitcnt lgkmcnt(7)\n\t"
    "v_mfma_f32_16x16x32_bf16 v[112:115], v[208:211], v[176:179], v[112:115]\n\t"
    "ds_read_b64_tr_b16 v[174:175], v241 offset:11712\n\t"
    "v_cvt_pk_bf16_f32 v18, v24, v25\n\t"
    "v_cvt_pk_bf16_f32 v19, v26, v27\n\t"
    "v_mfma_f32_16x16x32_bf16 v[116:119], v[212:215], v[176:179], v[116:119]\n\t"
    "ds_read_b64_tr_b16 v[176:177], v234 offset:192\n\t"
    "buffer_store_dwordx4 v[16:19], v244, s[72:75], s66 offen offset:1024 sc1\n\t"
    "v_cvt_pk_bf16_f32 v20, v20, v21\n\t"
    "v_mfma_f32_16x16x32_bf16 v[124:127], v[212:215], v[180:183], v[124:127]\n\t"
    "ds_read_b64_tr_b16 v[178:179], v234 offset:1472\n\t"
    "v_cvt_pk_bf16_f32 v21, v22, v23\n\t"
    "v_cvt_pk_bf16_f32 v22, v28, v29\n\t"
    "v_mfma_f32_16x16x32_bf16 v[120:123], v[208:211], v[180:183], v[120:123]\n\t"
    "ds_read_b64_tr_b16 v[180:181], v235 offset:192\n\t"
    "v_cvt_pk_bf16_f32 v23, v30, v31\n\t"
    "buffer_store_dwordx4 v[20:23], v244, s[76:79], s44 offen offset:1024 sc1\n\t"
    "v_mfma_f32_16x16x32_bf16 v[112:115], v[192:195], v[184:187], v[112:115]\n\t"
    "ds_read_b64_tr_b16 v[182:183], v235 offset:1472\n\t"
    "v_mfma_f32_16x16x32_bf16 v[116:119], v[196:199], v[184:187], v[116:119]\n\t"
    "ds_read_b64_tr_b16 v[184:185], v234 offset:10432\n\t"
    "v_mfma_f32_16x16x32_bf16 v[124:127], v[196:199], v[188:191], v[124:127]\n\t"
    "ds_read_b64_tr_b16 v[186:187], v234 offset:11712\n\t"
    "v_mfma_f32_16x16x32_bf16 v[120:123], v[192:195], v[188:191], v[120:123]\n\t"
    "ds_read_b64_tr_b16 v[188:189], v235 offset:10432\n\t"
    "s_waitcnt lgkmcnt(7)\n\t"
    "v_mfma_f32_16x16x32_bf16 v[128:131], v[200:203], v[160:163], v[128:131]\n\t"
    "ds_read_b64_tr_b16 v[190:191], v235 offset:11712\n\t"
    "v_cvt_pk_bf16_f32 v112, v112, v113\n\t"
    "v_cvt_pk_bf16_f32 v113, v114, v115\n\t"
    "v_mfma_f32_16x16x32_bf16 v[132:135], v[204:207], v[160:163], v[132:135]\n\t"
    "ds_read_b64_tr_b16 v[160:161], v240 offset:256\n\t"
    "v_cvt_pk_bf16_f32 v114, v116, v117\n\t"
    "v_cvt_pk_bf16_f32 v115, v118, v119\n\t"
    "v_mfma_f32_16x16x32_bf16 v[140:143], v[204:207], v[164:167], v[140:143]\n\t"
    "ds_read_b64_tr_b16 v[162:163], v240 offset:1536\n\t"
    "v_cvt_pk_bf16_f32 v120, v120, v121\n\t"
    "v_cvt_pk_bf16_f32 v121, v122, v123\n\t"
    "v_mfma_f32_16x16x32_bf16 v[136:139], v[200:203], v[164:167], v[136:139]\n\t"
    "ds_read_b64_tr_b16 v[164:165], v241 offset:256\n\t"
    "v_cvt_pk_bf16_f32 v122, v124, v125\n\t"
    "v_cvt_pk_bf16_f32 v123, v126, v127\n\t"
    "v_mfma_f32_16x16x32_bf16 v[128:131], v[224:227], v[168:171], v[128:131]\n\t"
    "ds_read_b64_tr_b16 v[166:167], v241 offset:1536\n\t"
    "v_mfma_f32_16x16x32_bf16 v[132:135], v[228:231], v[168:171], v[132:135]\n\t"
    "ds_read_b64_tr_b16 v[168:169], v240 offset:10496\n\t"
    "v_mfma_f32_16x16x32_bf16 v[32:35], v[112:115], v[216:219], v[32:35]\n\t"
    "ds_read_b64_tr_b16 v[170:171], v240 offset:11776\n\t"
    "v_mfma_f32_16x16x32_bf16 v[36:39], v[112:115], v[220:223], v[36:39]\n\t"
    "v_mfma_f32_16x16x32_bf16 v[44:47], v[120:123], v[220:223], v[44:47]\n\t"
    "v_mfma_f32_16x16x32_bf16 v[40:43], v[120:123], v[216:219], v[40:43]\n\t"
    "v_mfma_f32_16x16x32_bf16 v[140:143], v[228:231], v[172:175], v[140:143]\n\t"
    "v_mfma_f32_16x16x32_bf16 v[136:139], v[224:227], v[172:175], v[136:139]\n\t"
    "ds_read_b64_tr_b16 v[172:173], v241 offset:10496\n\t"
    "s_nop 0\n\t"
    "v_cvt_pk_bf16_f32 v32, v32, v33\n\t"
    "v_cvt_pk_bf16_f32 v33, v34, v35\n\t"
    "s_waitcnt lgkmcnt(7)\n\t"
    "v_mfma_f32_16x16x32_bf16 v[128:131], v[208:211], v[176:179], v[128:131]\n\t"
    "ds_read_b64_tr_b16 v[174:175], v241 offset:11776\n\t"
    "v_cvt_pk_bf16_f32 v34, v40, v41\n\t"
    "v_cvt_pk_bf16_f32 v35, v42, v43\n\t"
    "v_mfma_f32_16x16x32_bf16 v[132:135], v[212:215], v[176:179], v[132:135]\n\t"
    "ds_read_b64_tr_b16 v[176:177], v234 offset:256\n\t"
    "buffer_store_dwordx4 v[32:35], v244, s[72:75], s66 offen offset:2048 sc1\n\t"
    "v_cvt_pk_bf16_f32 v36, v36, v37\n\t"
    "v_mfma_f32_16x16x32_bf16 v[140:143], v[212:215], v[180:183], v[140:143]\n\t"
    "ds_read_b64_tr_b16 v[178:179], v234 offset:1536\n\t"
    "v_cvt_pk_bf16_f32 v37, v38, v39\n\t"
    "v_cvt_pk_bf16_f32 v38, v44, v45\n\t"
    "v_mfma_f32_16x16x32_bf16 v[136:139], v[208:211], v[180:183], v[136:139]\n\t"
    "ds_read_b64_tr_b16 v[180:181], v235 offset:256\n\t"
    "v_cvt_pk_bf16_f32 v39, v46, v47\n\t"
    "buffer_store_dwordx4 v[36:39], v244, s[76:79], s44 offen offset:2048 sc1\n\t"
    "v_mfma_f32_16x16x32_bf16 v[128:131], v[192:195], v[184:187], v[128:131]\n\t"
    "ds_read_b64_tr_b16 v[182:183], v235 offset:1536\n\t"
    "v_mfma_f32_16x16x32_bf16 v[132:135], v[196:199], v[184:187], v[132:135]\n\t"
    "ds_read_b64_tr_b16 v[184:185], v234 offset:10496\n\t"
    "v_mfma_f32_16x16x32_bf16 v[140:143], v[196:199], v[188:191], v[140:143]\n\t"
    "ds_read_b64_tr_b16 v[186:187], v234 offset:11776\n\t"
    "v_mfma_f32_16x16x32_bf16 v[136:139], v[192:195], v[188:191], v[136:139]\n\t"
    "ds_read_b64_tr_b16 v[188:189], v235 offset:10496\n\t"
    "s_waitcnt lgkmcnt(7)\n\t"
    "v_mfma_f32_16x16x32_bf16 v[144:147], v[200:203], v[160:163], v[144:147]\n\t"
    "ds_read_b64_tr_b16 v[190:191], v235 offset:11776\n\t"
    "v_cvt_pk_bf16_f32 v128, v128, v129\n\t"
    "v_cvt_pk_bf16_f32 v129, v130, v131\n\t"
    "v_mfma_f32_16x16x32_bf16 v[148:151], v[204:207], v[160:163], v[148:151]\n\t"
    "v_cvt_pk_bf16_f32 v130, v132, v133\n\t"
    "v_cvt_pk_bf16_f32 v131, v134, v135\n\t"
    "v_mfma_f32_16x16x32_bf16 v[156:159], v[204:207], v[164:167], v[156:159]\n\t"
    "v_cvt_pk_bf16_f32 v136, v136, v137\n\t"
    "v_cvt_pk_bf16_f32 v137, v138, v139\n\t"
    "v_mfma_f32_16x16x32_bf16 v[152:155], v[200:203], v[164:167], v[152:155]\n\t"
    "v_cvt_pk_bf16_f32 v138, v140, v141\n\t"
    "v_cvt_pk_bf16_f32 v139, v142, v143\n\t"
    "v_mfma_f32_16x16x32_bf16 v[144:147], v[224:227], v[168:171], v[144:147]\n\t"
    "v_mfma_f32_16x16x32_bf16 v[148:151], v[228:231], v[168:171], v[148:151]\n\t"
    "v_mfma_f32_16x16x32_bf16 v[48:51], v[128:131], v[216:219], v[48:51]\n\t"
    "v_mfma_f32_16x16x32_bf16 v[52:55], v[128:131], v[220:223], v[52:55]\n\t"
    "v_mfma_f32_16x16x32_bf16 v[60:63], v[136:139], v[220:223], v[60:63]\n\t"
    "v_mfma_f32_16x16x32_bf16 v[56:59], v[136:139], v[216:219], v[56:59]\n\t"
    "v_mfma_f32_16x16x32_bf16 v[156:159], v[228:231], v[172:175], v[156:159]\n\t"
    "v_mfma_f32_16x16x32_bf16 v[152:155], v[224:227], v[172:175], v[152:155]\n\t"
    "s_nop 2\n\t"
    "v_cvt_pk_bf16_f32 v48, v48, v49\n\t"
    "v_cvt_pk_bf16_f32 v49, v50, v51\n\t"
    "s_waitcnt lgkmcnt(6)\n\t"
    "v_mfma_f32_16x16x32_bf16 v[144:147], v[208:211], v[176:179], v[144:147]\n\t"
    "v_cvt_pk_bf16_f32 v50, v56, v57\n\t"
    "v_cvt_pk_bf16_f32 v51, v58, v59\n\t"
    "v_mfma_f32_16x16x32_bf16 v[148:151], v[212:215], v[176:179], v[148:151]\n\t"
    "buffer_store_dwordx4 v[48:51], v244, s[72:75], s66 offen offset:3072 sc1\n\t"
    "v_cvt_pk_bf16_f32 v52, v52, v53\n\t"
    "s_waitcnt lgkmcnt(4)\n\t"
    "v_mfma_f32_16x16x32_bf16 v[156:159], v[212:215], v[180:183], v[156:159]\n\t"
    "v_cvt_pk_bf16_f32 v53, v54, v55\n\t"
    "v_cvt_pk_bf16_f32 v54, v60, v61\n\t"
    "v_mfma_f32_16x16x32_bf16 v[152:155], v[208:211], v[180:183], v[152:155]\n\t"
    "v_cvt_pk_bf16_f32 v55, v62, v63\n\t"
    "buffer_store_dwordx4 v[52:55], v244, s[76:79], s44 offen offset:3072 sc1\n\t"
    "s_waitcnt lgkmcnt(2)\n\t"
    "v_mfma_f32_16x16x32_bf16 v[144:147], v[192:195], v[184:187], v[144:147]\n\t"
    "v_mfma_f32_16x16x32_bf16 v[148:151], v[196:199], v[184:187], v[148:151]\n\t"
    "s_waitcnt lgkmcnt(0)\n\t"
    "v_mfma_f32_16x16x32_bf16 v[156:159], v[196:199], v[188:191], v[156:159]\n\t"
    "v_mfma_f32_16x16x32_bf16 v[152:155], v[192:195], v[188:191], v[152:155]\n\t"
    "s_nop 3\n\t"
    "v_cvt_pk_bf16_f32 v144, v144, v145\n\t"
    "v_cvt_pk_bf16_f32 v145, v146, v147\n\t"
    "v_cvt_pk_bf16_f32 v146, v148, v149\n\t"
    "v_cvt_pk_bf16_f32 v147, v150, v151\n\t"
    "v_cvt_pk_bf16_f32 v152, v152, v153\n\t"
    "v_cvt_pk_bf16_f32 v153, v154, v155\n\t"
    "v_cvt_pk_bf16_f32 v154, v156, v157\n\t"
    "v_cvt_pk_bf16_f32 v155, v158, v159\n\t"
    "v_mfma_f32_16x16x32_bf16 v[64:67], v[144:147], v[216:219], v[64:67]\n\t"
    "v_mfma_f32_16x16x32_bf16 v[68:71], v[144:147], v[220:223], v[68:71]\n\t"
    "v_mfma_f32_16x16x32_bf16 v[76:79], v[152:155], v[220:223], v[76:79]\n\t"
    "v_mfma_f32_16x16x32_bf16 v[72:75], v[152:155], v[216:219], v[72:75]\n\t"
    "s_nop 4\n\t"
    "v_cvt_pk_bf16_f32 v64, v64, v65\n\t"
    "v_cvt_pk_bf16_f32 v65, v66, v67\n\t"
    "s_nop 0\n\t"
    "v_cvt_pk_bf16_f32 v66, v72, v73\n\t"
    "v_cvt_pk_bf16_f32 v67, v74, v75\n\t"
    "buffer_store_dwordx4 v[64:67], v244, s[72:75], s42 offen offset:0 sc1\n\t"
    "v_cvt_pk_bf16_f32 v68, v68, v69\n\t"
    "v_cvt_pk_bf16_f32 v69, v70, v71\n\t"
    "v_cvt_pk_bf16_f32 v70, v76, v77\n\t"
    "v_cvt_pk_bf16_f32 v71, v78, v79\n\t"
    "buffer_store_dwordx4 v[68:71], v244, s[76:79], s45 offen offset:0 sc1\n\t"
    "s_cmp_lt_u32 s71, 2\n\t"
    "s_cbranch_scc1 OWN%=\n\t"
    "OWNB%=:\n\t"
    "s_barrier\n\t"
    "s_waitcnt vmcnt(0)\n\t"
    "s_mov_b64 exec, 0x30\n\t"
    "buffer_store_dwordx2 v[248:249], v254, s[24:27], s67 offen offset:512 sc1\n\t"
    "s_mov_b64 exec, -1\n\t"
    "s_cmp_eq_u32 s37, 0\n\t"
    "s_cbranch_scc1 CW0%=\n\t"
    "s_endpgm\n\t"
    "CW0%=:\n\t"
    "s_mov_b32 s56, 4\n\t"
    "s_lshl_b32 s57, s36, 13\n\t"
    "s_add_u32 s57, s57, 66048\n\t"
    "v_and_b32 v90, 7, v251\n\t"
    "v_lshlrev_b32 v90, 10, v90\n\t"
    "s_mul_i32 s58, s36, 40960\n\t"
    "s_lshl_b32 s38, s56, 10\n\t"
    "s_add_u32 s58, s58, s38\n\t"
    "s_add_u32 s58, s58, 4259840\n\t"
    "v_lshlrev_b32 v91, 4, v251\n\t"
    "s_add_u32 s38, s34, 1\n\t"
    "s_and_b32 s38, s38, 7\n\t"
    "s_mul_i32 s38, s38, 5120\n\t"
    "s_add_u32 s61, s58, s38\n\t"
    "s_add_u32 s38, s34, 2\n\t"
    "s_and_b32 s38, s38, 7\n\t"
    "s_mul_i32 s38, s38, 5120\n\t"
    "s_add_u32 s62, s58, s38\n\t"
    "s_add_u32 s38, s34, 3\n\t"
    "s_and_b32 s38, s38, 7\n\t"
    "s_mul_i32 s38, s38, 5120\n\t"
    "s_add_u32 s63, s58, s38\n\t"
    "s_add_u32 s38, s34, 4\n\t"
    "s_and_b32 s38, s38, 7\n\t"
    "s_mul_i32 s38, s38, 5120\n\t"
    "s_add_u32 s64, s58, s38\n\t"
    "s_add_u32 s38, s34, 5\n\t"
    "s_and_b32 s38, s38, 7\n\t"
    "s_mul_i32 s38, s38, 5120\n\t"
    "s_add_u32 s65, s58, s38\n\t"
    "s_add_u32 s38, s34, 6\n\t"
    "s_and_b32 s38, s38, 7\n\t"
    "s_mul_i32 s38, s38, 5120\n\t"
    "s_add_u32 s66, s58, s38\n\t"
    "s_add_u32 s38, s34, 7\n\t"
    "s_and_b32 s38, s38, 7\n\t"
    "s_mul_i32 s38, s38, 5120\n\t"
    "s_add_u32 s67, s58, s38\n\t"
    "s_lshl_b32 s38, s56, 10\n\t"
    "s_cmp_eq_u32 s56, 4\n\t"
    "s_cselect_b32 s38, 40960, s38\n\t"
    "s_add_u32 s38, s38, 36864\n\t"
    "v_add_u32 v98, s38, v91\n\t"
    "s_lshl_b32 s38, s29, 3\n\t"
    "s_add_u32 s38, s38, s34\n\t"
    "s_mul_i32 s38, s38, 40960\n\t"
    "s_mul_i32 s39, s19, 1280\n\t"
    "s_add_u32 s38, s38, s39\n\t"
    "s_mul_i32 s39, s35, 320\n\t"
    "s_add_u32 s38, s38, s39\n\t"
    "s_lshl_b32 s39, s56, 6\n\t"
    "s_add_u32 s38, s38, s39\n\t"
    "s_add_u32 s20, s20, s38\n\t"
    "s_addc_u32 s21, s21, 0\n\t"
    "v_and_b32 v92, 15, v251\n\t"
    "v_mul_u32_u24 v92, 0xa00, v92\n\t"
    "v_lshrrev_b32 v93, 4, v251\n\t"
    "v_lshl_add_u32 v92, v93, 3, v92\n\t"
    "v_and_b32 v93, 1, v93\n\t"
    "v_mul_u32_u24 v93, 24, v93\n\t"
    "v_add_u32 v99, v92, v93\n\t"
    "s_mov_b32 s59, 0x40000\n\t"
    "s_branch RED%=\n\t"
    "PACE%=:\n\t"
    "s_and_b32 s58, s57, 15\n\t"
    "s_lshr_b32 s57, s57, 4\n\t"
    "PACEL%=:\n\t"
    "s_sub_u32 s58, s58, 1\n\t"
    "s_cbranch_scc1 PACEB%=\n\t"
    "s_sleep 1\n\t"
    "s_branch PACEL%=\n\t"
    "PICK%=:\n\t"
    "ds_read_b64 v[64:65], v20 offset:0\n\t"
    "ds_read_b64 v[66:67], v21 offset:0\n\t"
    "ds_read_b64 v[68:69], v20 offset:2048\n\t"
    "ds_read_b64 v[70:71], v21 offset:2048\n\t"
    "ds_read_b64 v[72:73], v22 offset:0\n\t"
    "ds_read_b64 v[74:75], v23 offset:0\n\t"
    "ds_read_b64 v[76:77], v22 offset:2048\n\t"
    "ds_read_b64 v[78:79], v23 offset:2048\n\t"
    "s_branch PICKB%=\n\t"
    "OWN%=:\n\t"
    "v_add_u32 v232, 36864, v244\n\t"
    "s_cmp_eq_u32 s71, 1\n\t"
    "s_cbranch_scc1 OWN1%=\n\t"
    "ds_write_b128 v232, v[0:3] offset:0\n\t"
    "ds_write_b128 v232, v[16:19] offset:1024\n\t"
    "ds_write_b128 v232, v[32:35] offset:2048\n\t"
    "ds_write_b128 v232, v[48:51] offset:3072\n\t"
    "ds_write_b128 v232, v[64:67] offset:40960\n\t"
    "s_waitcnt lgkmcnt(0)\n\t"
    "s_branch OWNB%=\n\t"
    "OWN1%=:\n\t"
    "ds_write_b128 v232, v[4:7] offset:0\n\t"
    "ds_write_b128 v232, v[20:23] offset:1024\n\t"
    "ds_write_b128 v232, v[36:39] offset:2048\n\t"
    "ds_write_b128 v232, v[52:55] offset:3072\n\t"
    "ds_write_b128 v232, v[68:71] offset:40960\n\t"
    "s_waitcnt lgkmcnt(0)\n\t"
    "s_branch OWNB%=\n\t"
    "DONE%=:\n\t"
    "s_nop 0"
    :
    : [ka] "s"(__builtin_amdgcn_kernarg_segment_ptr()), [bid] "s"(blockIdx.x), [tid] "v"(threadIdx.x)
    : "memory", "scc", "vcc", "s8", "s9", "s10", "s11", "s12", "s13", "s14", "s15", "s16", "s17", "s18", "s19", "s20", "s21", "s22", "s23", "s24", "s25", "s26", "s27", "s28", "s29", "s30", "s31", "s34", "s35", "s36", "s37", "s38", "s39", "s40", "s41", "s42", "s43", "s44", "s45", "s46", "s47", "s48", "s49", "s50", "s51", "s52", "s53", "s54", "s55", "s56", "s57", "s58", "s59", "s60", "s61", "s62", "s63", "s64", "s65", "s66", "s67", "s68", "s69", "s70", "s71", "s72", "s73", "s74", "s75", "s76", "s77", "s78", "s79", "s80", "s81", "s82", "s83", "s84", "s85", "s86", "s87", "s88", "s89", "s90", "s91", "s92", "s93", "s94", "s95", "v0", "v1", "v2", "v3", "v4", "v5", "v6", "v7", "v8", "v9", "v10", "v11", "v12", "v13", "v14", "v15", "v16", "v17", "v18", "v19", "v20", "v21", "v22", "v23", "v24", "v25", "v26", "v27", "v28", "v29", "v30", "v31", "v32", "v33", "v34", "v35", "v36", "v37", "v38", "v39", "v40", "v41", "v42", "v43", "v44", "v45", "v46", "v47", "v48", "v49", "v50", "v51", "v52", "v53", "v54", "v55", "v56", "v57", "v58", "v59", "v60", "v61", "v62", "v63", "v64", "v65", "v66", "v67", "v68", "v69", "v70", "v71", "v72", "v73", "v74", "v75", "v76", "v77", "v78", "v79", "v80", "v81", "v82", "v83", "v84", "v85", "v86", "v87", "v88", "v89", "v90", "v91", "v92", "v93", "v94", "v95", "v96", "v97", "v98", "v99", "v100", "v101", "v102", "v103", "v104", "v105", "v106", "v107", "v108", "v109", "v110", "v111", "v112", "v113", "v114", "v115", "v116", "v117", "v118", "v119", "v120", "v121", "v122", "v123", "v124", "v125", "v126", "v127", "v128", "v129", "v130", "v131", "v132", "v133", "v134", "v135", "v136", "v137", "v138", "v139", "v140", "v141", "v142", "v143", "v144", "v145", "v146", "v147", "v148", "v149", "v150", "v151", "v152", "v153", "v154", "v155", "v156", "v157", "v158", "v159", "v160", "v161", "v162", "v163", "v164", "v165", "v166", "v167", "v168", "v169", "v170", "v171", "v172", "v173", "v174", "v175", "v176", "v177", "v178", "v179", "v180", "v181", "v182", "v183", "v184", "v185", "v186", "v187", "v188", "v189", "v190", "v191", "v192", "v193", "v194", "v195", "v196", "v197", "v198", "v199", "v200", "v201", "v202", "v203", "v204", "v205", "v206", "v207", "v208", "v209", "v210", "v211", "v212", "v213", "v214", "v215", "v216", "v217", "v218", "v219", "v220", "v221", "v222", "v223", "v224", "v225", "v226", "v227", "v228", "v229", "v230", "v231", "v232", "v233", "v234", "v235", "v236", "v237", "v238", "v239", "v240", "v241", "v242", "v243", "v244", "v245", "v246", "v247", "v248", "v249", "v250", "v251", "v252", "v253", "v254");
}

extern "C" void kernel_launch(void* const* d_in, const int* in_sizes, int n_in, void* d_out, int out_size,
                              void* d_ws, size_t ws_size, hipStream_t stream) {
  Args a;
  for (int i = 0; i < 10; ++i) a.in[i] = d_in[i];
  a.out = d_out;
  a.ws = d_ws;
  (void)hipFuncSetAttribute((const void*)fused_attn_block, hipFuncAttributeMaxDynamicSharedMemorySize, LDS_BYTES);
  fused_attn_block<<<256, 512, LDS_BYTES, stream>>>(a);
}
```
